```python
import torch

def _softmax(x, axis=-1):
    return torch.softmax(x, dim=axis)
def _gelu(x):
    return torch.nn.functional.gelu(x, approximate="tanh")
def _silu(x):
    return torch.nn.functional.silu(x)
def _sigmoid(x):
    return torch.sigmoid(x)
def _layernorm(x, axis=-1, eps=1e-5):
    m = x.mean(dim=axis, keepdim=True)
    v = x.var(dim=axis, keepdim=True, unbiased=False)
    return (x - m) / torch.sqrt(v + eps)
def _scatter_add(tgt, idx, src, axis=0):
    return tgt.clone().index_add_(axis, idx, src)
def _segment_sum(src, seg, n):
    out = torch.zeros((n,) + src.shape[1:], dtype=src.dtype)
    return out.index_add_(0, seg, src)
def _pad(x, pw):
    flat = tuple(v for pair in reversed(pw) for v in pair)
    return torch.nn.functional.pad(x, flat)

kernel_name = 'standalone__paged_block_table_kv_pagedattention__decode'

def setup_inputs():

    _w = (lambda a: (lambda t: t.to(torch.bfloat16) if t.is_floating_point() else t)(torch.as_tensor(a)))
    B, Hq, Hkv, Sq, Skv, D = 1, 32, 32, 1, 4096, 128
    bs = 16
    nbps = max(1, Skv // bs)
    num_phys = B * nbps + 4
    bt = torch.randperm(num_phys)[: B * nbps].reshape(B, nbps).to(torch.int64)
    cl = (nbps * bs - (torch.arange(1, B + 1, dtype=torch.int64) * 2654435761) % max(1, nbps * bs // 2)).to(torch.int64)
    return {
        "q": _w((lambda *s: torch.randn(*s, dtype=torch.bfloat16) * 0.8)(B, Hq, Sq, D)),
        "k_cache": _w((lambda *s: torch.randn(*s, dtype=torch.bfloat16) * 0.8)(num_phys, bs, Hkv, D)),
        "v_cache": _w((lambda *s: torch.randn(*s, dtype=torch.bfloat16) * 0.05)(num_phys, bs, Hkv, D)),
        "block_table": _w(bt),
        "context_lens": _w(cl),
    }

def reference(q, k_cache, v_cache, block_table, context_lens):
    B, Hq, Sq, D = q.shape
    bs = k_cache.shape[1]
    Hkv = k_cache.shape[2]
    nbps = block_table.shape[1]
    Skv = nbps * bs
    bt = block_table.reshape(-1)
    k = (lambda x, *d: x.permute(*d))(k_cache[bt].reshape(B, Skv, Hkv, D), 0, 2, 1, 3)
    v = (lambda x, *d: x.permute(*d))(v_cache[bt].reshape(B, Skv, Hkv, D), 0, 2, 1, 3)
    g = Hq // Hkv
    qg = q.reshape(B, Hkv, g, Sq, D)
    scores = torch.einsum("bhgqd,bhkd->bhgqk", qg, k) * (D ** -0.5)
    pos = torch.arange(Skv)
    msk = pos[None, :] < context_lens[:, None]
    scores = torch.where(msk[:, None, None, None, :], scores, -1e9)
    p = _softmax(scores, axis=-1)
    out = torch.einsum("bhgqk,bhkd->bhgqd", p, v)
    return out.reshape(B, Hq, Sq, D)
```

```cpp
#include <hip/hip_runtime.h>
#include <hip/hip_bf16.h>
#include <stdint.h>

#define CFG_W 8
#define CFG_COMBINE 1
#define CFG_NT 2
#define CFG_PIN 0

constexpr int H = 32, D = 128, PAGE = 16, NBPS = 256, SKV = NBPS * PAGE;
constexpr int S = 8;
constexpr int W = CFG_W;
constexpr int PPW = NBPS / (S * W);
constexpr int NU = PPW * 4;
constexpr unsigned TOKB = H * D * 2;
constexpr unsigned PAGEB = PAGE * TOKB;
constexpr float C2 = 0.088388347648318440f * 1.4426950408889634f;
constexpr float NEGBIG2 = -1.0e9f * 1.4426950408889634f;

typedef unsigned u32x4 __attribute__((ext_vector_type(4)));
typedef unsigned u32x2 __attribute__((ext_vector_type(2)));
typedef float f32x2 __attribute__((ext_vector_type(2)));
typedef __bf16 bf16x2 __attribute__((ext_vector_type(2)));
#define GAS __attribute__((address_space(1)))
typedef GAS unsigned gu32;

__device__ __forceinline__ float dot2(unsigned a, unsigned b, float c) {
  return __builtin_amdgcn_fdot2_f32_bf16(__builtin_bit_cast(bf16x2, a), __builtin_bit_cast(bf16x2, b), c, false);
}
template <int CTRL> __device__ __forceinline__ float dpp(float x) {
  return __builtin_bit_cast(float, __builtin_amdgcn_mov_dpp(__builtin_bit_cast(int, x), CTRL, 0xf, 0xf, true));
}
__device__ __forceinline__ float rowsum16(float x) {
  x += dpp<0x128>(x); x += dpp<0x124>(x); x += dpp<0x122>(x); x += dpp<0x121>(x); return x;
}
__device__ __forceinline__ float xrow_max(float x) {
  auto s = __builtin_amdgcn_permlane16_swap(__float_as_uint(x), __float_as_uint(x), false, false);
  x = fmaxf(__uint_as_float(s[0]), __uint_as_float(s[1]));
  auto t = __builtin_amdgcn_permlane32_swap(__float_as_uint(x), __float_as_uint(x), false, false);
  return fmaxf(__uint_as_float(t[0]), __uint_as_float(t[1]));
}
__device__ __forceinline__ float xrow_sum(float x) {
  auto s = __builtin_amdgcn_permlane16_swap(__float_as_uint(x), __float_as_uint(x), false, false);
  x = __uint_as_float(s[0]) + __uint_as_float(s[1]);
  auto t = __builtin_amdgcn_permlane32_swap(__float_as_uint(x), __float_as_uint(x), false, false);
  return __uint_as_float(t[0]) + __uint_as_float(t[1]);
}
__device__ __forceinline__ unsigned pack_bf16(float a, float b) {
  bf16x2 r; r[0] = (__bf16)a; r[1] = (__bf16)b; return __builtin_bit_cast(unsigned, r);
}
__device__ __forceinline__ float rdl(float x, int l) {
  return __builtin_bit_cast(float, __builtin_amdgcn_readlane(__builtin_bit_cast(int, x), l));
}

constexpr unsigned WS_EPOCH = 0, WS_ML = 8192, WS_REC = 16384, RECB = 1024;
constexpr unsigned WS_BYTES = WS_REC + H * S * RECB;

#define STAMP(i)
#define STAMP_FLUSH()
__device__ __forceinline__ void masked_tail(const unsigned short* v, const int* bt, int len, int h, int lane,
                                         float& M, float& L, float& o0, float& o1) {
  const float Mn = fmaxf(M, NEGBIG2);
  const float a = (L > 0.f) ? __builtin_amdgcn_exp2f(M - Mn) : 0.f;
  const float wm = __builtin_amdgcn_exp2f(NEGBIG2 - Mn);
  L *= a; o0 *= a; o1 *= a;
  float s0 = 0.f, s1 = 0.f;
  for (int j = len; j < SKV; ++j) {
    const unsigned pg = (unsigned)bt[j >> 4];
    const unsigned x = *reinterpret_cast<const unsigned*>(v + (size_t)pg * (PAGEB / 2) + (size_t)(j & 15) * (TOKB / 2) + h * D + 2 * lane);
    s0 += __uint_as_float(x << 16); s1 += __uint_as_float(x & 0xffff0000u);
  }
  L += wm * (float)(SKV - len); o0 += wm * s0; o1 += wm * s1; M = Mn;
}

struct KArgs { const unsigned short* q; const unsigned short* k; const unsigned short* v; const int* bt; const int* lens; unsigned* out; unsigned char* ws; unsigned long long kv_bytes; };
typedef __attribute__((address_space(4))) const KArgs* KArgsP;
typedef __attribute__((address_space(4))) const int* CIntP;
__global__ void __launch_bounds__(W * 64, W / 4) attn_kernel(KArgs unused_args) {
  __shared__ __attribute__((aligned(16))) float lds[W * (D + 4)];
  STAMP(0);
  KArgsP ka = (KArgsP)__builtin_amdgcn_kernarg_segment_ptr();
  asm volatile("" : "+s"(ka));
  const unsigned short* q = ka->q; const unsigned short* k = ka->k; const unsigned short* v = ka->v;
  CIntP bt = (CIntP)ka->bt; CIntP lens = (CIntP)ka->lens; unsigned* out = ka->out; unsigned char* ws = ka->ws;
  const unsigned kv_bytes = (unsigned)ka->kv_bytes;
#define CFG_HROT 0
  const int s = blockIdx.x & (S - 1), h = (blockIdx.x / S + CFG_HROT) & (H - 1);
  const int lane = threadIdx.x & 63, wave = __builtin_amdgcn_readfirstlane(threadIdx.x >> 6);
  const int r = lane >> 4, c = lane & 15;

  int len = lens[0];
  len = min(max(len, 0), SKV);
  int pg[PPW];
#pragma unroll
  for (int i = 0; i < PPW; ++i) pg[i] = bt[i * (S * W) + wave * S + s];
  const u32x4 qv = *reinterpret_cast<const u32x4*>(q + h * D + 8 * c);
  const unsigned tag = __hip_atomic_load((gu32*)(ws + WS_EPOCH + h * 256), __ATOMIC_RELAXED, __HIP_MEMORY_SCOPE_AGENT) + 1u;

  const auto krs = __builtin_amdgcn_make_buffer_rsrc((void*)k, 0, kv_bytes, 0x00020000);
  const auto vrs = __builtin_amdgcn_make_buffer_rsrc((void*)v, 0, kv_bytes, 0x00020000);
  const unsigned voff = (unsigned)r * TOKB + (unsigned)h * (D * 2) + (unsigned)c * 16;
  unsigned soff[PPW];
#pragma unroll
  for (int i = 0; i < PPW; ++i) {
    const int lp = i * (S * W) + wave * S + s;
    soff[i] = (lp * PAGE < len) ? (unsigned)pg[i] * PAGEB : 0x80000000u;
  }
  u32x4 K[NU], V[NU];
  STAMP(1);
#pragma unroll
  for (int u = 0; u < NU; ++u) K[u] = __builtin_amdgcn_raw_buffer_load_b128(krs, voff, soff[u >> 2] + (u & 3) * 4 * TOKB, CFG_NT);
#pragma unroll
  for (int u = 0; u < NU; ++u) V[u] = __builtin_amdgcn_raw_buffer_load_b128(vrs, voff, soff[u >> 2] + (u & 3) * 4 * TOKB, CFG_NT);

  __builtin_amdgcn_sched_barrier(0);
  STAMP(3);
  float sc[NU];
  float m = -INFINITY;
#pragma unroll
  for (int u = 0; u < NU; ++u) {
    float d = dot2(K[u][0], qv[0], 0.f);
    d = dot2(K[u][1], qv[1], d); d = dot2(K[u][2], qv[2], d); d = dot2(K[u][3], qv[3], d);
    d = rowsum16(d);
    const int tok = ((u >> 2) * (S * W) + wave * S + s) * PAGE + (u & 3) * 4 + r;
    sc[u] = (tok < len) ? d * C2 : -INFINITY;
    m = fmaxf(m, sc[u]);
  }
  m = xrow_max(m);
  STAMP(4);
  const float msub = (m == -INFINITY) ? 0.f : m;
  float l = 0.f, acc[8];
#pragma unroll
  for (int j = 0; j < 8; ++j) acc[j] = 0.f;
#pragma unroll
  for (int u = 0; u < NU; ++u) { sc[u] = __builtin_amdgcn_exp2f(sc[u] - msub); l += sc[u]; }
  l = xrow_sum(l);
#pragma unroll
  for (int u = 0; u < NU; ++u) {
#pragma unroll
    for (int j = 0; j < 4; ++j) {
      acc[2 * j] = fmaf(sc[u], __uint_as_float(V[u][j] << 16), acc[2 * j]);
      acc[2 * j + 1] = fmaf(sc[u], __uint_as_float(V[u][j] & 0xffff0000u), acc[2 * j + 1]);
    }
  }
  STAMP(5);
  float b4[4];
#pragma unroll
  for (int j = 0; j < 4; ++j) {
    auto t = __builtin_amdgcn_permlane32_swap(__float_as_uint(acc[j]), __float_as_uint(acc[j + 4]), false, false);
    b4[j] = __uint_as_float(t[0]) + __uint_as_float(t[1]);
  }
  float b2[2];
#pragma unroll
  for (int j = 0; j < 2; ++j) {
    auto t = __builtin_amdgcn_permlane16_swap(__float_as_uint(b4[j]), __float_as_uint(b4[j + 2]), false, false);
    b2[j] = __uint_as_float(t[0]) + __uint_as_float(t[1]);
  }
  float* rec = lds + wave * (D + 4);
  *reinterpret_cast<f32x2*>(rec + 4 + 8 * c + 2 * r) = f32x2{b2[0], b2[1]};
  if (lane == 0) *reinterpret_cast<f32x2*>(rec) = f32x2{m, l};
  __syncthreads();
  STAMP(6);
  if (wave != 0) { STAMP_FLUSH(); return; }
  float M = -INFINITY;
  float mw[W], lw[W];
#pragma unroll
  for (int w = 0; w < W; ++w) { const f32x2 t = *reinterpret_cast<const f32x2*>(lds + w * (D + 4)); mw[w] = t[0]; lw[w] = t[1]; M = fmaxf(M, t[0]); }
  const float Ms = (M == -INFINITY) ? 0.f : M;
  float L = 0.f, o0 = 0.f, o1 = 0.f;
#pragma unroll
  for (int w = 0; w < W; ++w) {
    const float wt = __builtin_amdgcn_exp2f(mw[w] - Ms);
    const f32x2 a = *reinterpret_cast<const f32x2*>(lds + w * (D + 4) + 4 + 2 * lane);
    L = fmaf(wt, lw[w], L); o0 = fmaf(wt, a[0], o0); o1 = fmaf(wt, a[1], o1);
  }
  const auto wrs = __builtin_amdgcn_make_buffer_rsrc((void*)ws, 0, WS_BYTES, 0x00020000);
  if (s != S - 1) {
    __builtin_amdgcn_raw_buffer_store_b128(u32x4{__float_as_uint(o0), tag, __float_as_uint(o1), tag}, wrs, lane * 16, WS_REC + (h * S + s) * RECB, 16);
    if (lane == 0) __builtin_amdgcn_raw_buffer_store_b128(u32x4{__float_as_uint(M), tag, __float_as_uint(L), tag}, wrs, 0, WS_ML + (h * S + s) * 16, 16);
    STAMP(7);
    STAMP_FLUSH();
    return;
  }
  u32x4 g[S - 1], gml;
  const int sl = min(lane, S - 2);
  for (unsigned spin = 0; spin < (1u << 20); ++spin) {
    gml = __builtin_amdgcn_raw_buffer_load_b128(wrs, sl * 16, WS_ML + h * S * 16, 16);
#pragma unroll
    for (int j = 0; j < S - 1; ++j) g[j] = __builtin_amdgcn_raw_buffer_load_b128(wrs, lane * 16, WS_REC + (h * S + j) * RECB, 16);
    bool ok = (gml[1] == tag) & (gml[3] == tag);
#pragma unroll
    for (int j = 0; j < S - 1; ++j) ok &= (g[j][1] == tag) & (g[j][3] == tag);
    if (__all(ok)) break;
    asm volatile("" ::: "memory");
  }
  if (lane == 0) __hip_atomic_store((gu32*)(ws + WS_EPOCH + h * 256), tag, __ATOMIC_RELAXED, __HIP_MEMORY_SCOPE_AGENT);
  float G = M;
  float ms[S - 1], ls[S - 1];
#pragma unroll
  for (int j = 0; j < S - 1; ++j) { ms[j] = rdl(__uint_as_float(gml[0]), j); ls[j] = rdl(__uint_as_float(gml[2]), j); G = fmaxf(G, ms[j]); }
  const float Gs = (G == -INFINITY) ? 0.f : G;
  float wt = __builtin_amdgcn_exp2f(M - Gs);
  L *= wt; o0 *= wt; o1 *= wt;
#pragma unroll
  for (int j = 0; j < S - 1; ++j) {
    wt = __builtin_amdgcn_exp2f(ms[j] - Gs);
    L = fmaf(wt, ls[j], L); o0 = fmaf(wt, __uint_as_float(g[j][0]), o0); o1 = fmaf(wt, __uint_as_float(g[j][2]), o1);
  }
  if (len < SKV && !(G > NEGBIG2 + 256.f)) masked_tail(v, (const int*)bt, len, h, lane, G, L, o0, o1);
  const float inv = 1.f / L;
  out[h * (D / 2) + lane] = pack_bf16(o0 * inv, o1 * inv);
  STAMP(7);
  STAMP_FLUSH();
}

extern "C" void kernel_launch(void* const* d_in, const int* in_sizes, int n_in, void* d_out, int out_size, void* d_ws,
                              size_t ws_size, hipStream_t stream) {
  const unsigned kv_bytes = (unsigned)in_sizes[1] * 2u;
  KArgs a{(const unsigned short*)d_in[0], (const unsigned short*)d_in[1], (const unsigned short*)d_in[2], (const int*)d_in[3],
          (const int*)d_in[4], (unsigned*)d_out, (unsigned char*)d_ws, (unsigned long long)kv_bytes};
  attn_kernel<<<dim3(H * S), dim3(W * 64), 0, stream>>>(a);
}
```

```cpp
#include <hip/hip_runtime.h>
#include <hip/hip_bf16.h>
#include <stdint.h>
#include <float.h>
#define CFG_PF 0
#define CFG_EW "4"

#define CFG_NT 2

constexpr int H = 32, D = 128, PAGE = 16, NBPS = 256, SKV = NBPS * PAGE;
constexpr int W = 8;
constexpr int NWG = 256;
constexpr int HG = 4;
constexpr int NG = H / HG;
constexpr int RPG = NWG / NG;
constexpr int NU = 16;
constexpr unsigned TOKB = H * D * 2;
constexpr unsigned PAGEB = PAGE * TOKB;
constexpr float C2 = 0.088388347648318440f * 1.4426950408889634f;
constexpr float NEGBIG2 = -1.0e9f * 1.4426950408889634f;

typedef unsigned u32x4 __attribute__((ext_vector_type(4)));
typedef unsigned u32x2 __attribute__((ext_vector_type(2)));
typedef float f32x2 __attribute__((ext_vector_type(2)));
typedef float f32x4 __attribute__((ext_vector_type(4)));
typedef __bf16 bf16x2 __attribute__((ext_vector_type(2)));
#define GAS __attribute__((address_space(1)))
typedef GAS unsigned gu32;

__device__ __forceinline__ float dot2(unsigned a, unsigned b, float c) {
  return __builtin_amdgcn_fdot2_f32_bf16(__builtin_bit_cast(bf16x2, a), __builtin_bit_cast(bf16x2, b), c, false);
}
template <int CTRL> __device__ __forceinline__ float dpp(float x) {
  return __builtin_bit_cast(float, __builtin_amdgcn_mov_dpp(__builtin_bit_cast(int, x), CTRL, 0xf, 0xf, true));
}
__device__ __forceinline__ float rowsum16(float x) {
  x += dpp<0x128>(x); x += dpp<0x124>(x); x += dpp<0x122>(x); x += dpp<0x121>(x); return x;
}
__device__ __forceinline__ float xrow_max(float x) {
  auto s = __builtin_amdgcn_permlane16_swap(__float_as_uint(x), __float_as_uint(x), false, false);
  x = fmaxf(__uint_as_float(s[0]), __uint_as_float(s[1]));
  auto t = __builtin_amdgcn_permlane32_swap(__float_as_uint(x), __float_as_uint(x), false, false);
  return fmaxf(__uint_as_float(t[0]), __uint_as_float(t[1]));
}
__device__ __forceinline__ float xrow_sum(float x) {
  auto s = __builtin_amdgcn_permlane16_swap(__float_as_uint(x), __float_as_uint(x), false, false);
  x = __uint_as_float(s[0]) + __uint_as_float(s[1]);
  auto t = __builtin_amdgcn_permlane32_swap(__float_as_uint(x), __float_as_uint(x), false, false);
  return __uint_as_float(t[0]) + __uint_as_float(t[1]);
}
__device__ __forceinline__ unsigned pack_bf16(float a, float b) {
  bf16x2 r; r[0] = (__bf16)a; r[1] = (__bf16)b; return __builtin_bit_cast(unsigned, r);
}

constexpr unsigned WS_EPOCH = 0, WS_REC = 4096;
constexpr unsigned WS_BYTES = WS_REC + NG * RPG * RPG * 128;
constexpr unsigned SENT = 0xBF800000u;

#define STAMP(i)
#define STAMP_FLUSH()
#define KEEPV(x)

struct KArgs { const unsigned short* q; const unsigned short* k; const unsigned short* v; const int* bt; const int* lens; unsigned* out; unsigned char* ws; unsigned long long kv_bytes; };
typedef __attribute__((address_space(4))) const KArgs* KArgsP;
typedef __attribute__((address_space(4))) const int* CIntP;
typedef unsigned long long u64;

__global__ void __launch_bounds__(W * 64, W / 4) attn_kernel(KArgs unused_args) {
  __shared__ __attribute__((aligned(16))) float lds_acc[W * HG * D];
  __shared__ __attribute__((aligned(16))) u64 lds_ml[HG * W];
  STAMP(0);
  KArgsP ka = (KArgsP)__builtin_amdgcn_kernarg_segment_ptr();
  asm volatile("" : "+s"(ka));
  const unsigned short* q = ka->q; const unsigned short* k = ka->k; const unsigned short* v = ka->v;
  CIntP bt = (CIntP)ka->bt; CIntP lens = (CIntP)ka->lens; unsigned* out = ka->out; unsigned char* ws = ka->ws;
  unsigned long long kvb = ka->kv_bytes;

  const int x = blockIdx.x & 7, g = (blockIdx.x >> 3) & 7, part = blockIdx.x >> 6;
  const int rank = part * 8 + x;
  const int lane = threadIdx.x & 63, wave = __builtin_amdgcn_readfirstlane(threadIdx.x >> 6);
  const int r = lane >> 4;

  if (threadIdx.x < HG * W) __hip_atomic_store(&lds_ml[threadIdx.x], (u64)SENT << 32, __ATOMIC_RELAXED, __HIP_MEMORY_SCOPE_WORKGROUP);
  unsigned lpb = (unsigned)((wave * 2 + (part >> 1)) * 8 + x) * 4u;
  asm volatile("" : "+s"(lpb));
  asm volatile("s_waitcnt lgkmcnt(0)" : "+s"(bt), "+s"(lens), "+s"(kvb) :: "memory");
  const unsigned kv_bytes = (unsigned)kvb;
  int len = lens[0];
  int lp[2], pg[2];
#pragma unroll
  for (int i = 0; i < 2; ++i) { lp[i] = (int)(lpb >> 2) + i * 128; pg[i] = *(CIntP)((const __attribute__((address_space(4))) char*)bt + lpb + i * 512); }
  u32x4 qv;
  unsigned tag;
  asm volatile("global_load_dwordx4 %0, %2, %3\n\tglobal_load_dword %1, %4, %5 sc1"
               : "=&v"(qv), "=&v"(tag) : "v"((unsigned)(g * (HG * D * 2) + lane * 16)), "s"(q), "v"((unsigned)(WS_EPOCH + g * 256)), "s"(ws) : "memory");
  unsigned voff[8];
#pragma unroll
  for (int t = 0; t < 8; ++t) {
    voff[t] = (unsigned)g * (HG * D * 2) + (unsigned)lane * 16 + (unsigned)((part & 1) * 8 + t) * TOKB;
    asm volatile("" : "+v"(voff[t]));
  }
  __builtin_amdgcn_sched_barrier(0);
  __builtin_amdgcn_s_barrier();
  __builtin_amdgcn_sched_barrier(0);
  int nl[2];
  unsigned soff[2], nrec[2];
#pragma unroll
  for (int i = 0; i < 2; ++i) {
    int n = max(len, lp[i] * PAGE) - lp[i] * PAGE;
    asm volatile("" : "+s"(n));
    nl[i] = min(n, PAGE);
    soff[i] = (unsigned)pg[i] * PAGEB;
    nrec[i] = min(kv_bytes, soff[i] + (unsigned)nl[i] * TOKB);
  }
  u32x4 K[NU], V[NU];
  STAMP(1);
#define LD8(pre, post, OUTC, X, i, rs) asm volatile(pre \
      "buffer_load_dwordx4 %0, %8, %16, %17 offen nt\n\tbuffer_load_dwordx4 %1, %9, %16, %17 offen nt\n\t" \
      "buffer_load_dwordx4 %2, %10, %16, %17 offen nt\n\tbuffer_load_dwordx4 %3, %11, %16, %17 offen nt\n\t" \
      "buffer_load_dwordx4 %4, %12, %16, %17 offen nt\n\tbuffer_load_dwordx4 %5, %13, %16, %17 offen nt\n\t" \
      "buffer_load_dwordx4 %6, %14, %16, %17 offen nt\n\tbuffer_load_dwordx4 %7, %15, %16, %17 offen nt\n\t" post \
      : OUTC(X[i * 8 + 0]), OUTC(X[i * 8 + 1]), OUTC(X[i * 8 + 2]), OUTC(X[i * 8 + 3]), OUTC(X[i * 8 + 4]), OUTC(X[i * 8 + 5]), OUTC(X[i * 8 + 6]), OUTC(X[i * 8 + 7]) \
      : "v"(voff[0]), "v"(voff[1]), "v"(voff[2]), "v"(voff[3]), "v"(voff[4]), "v"(voff[5]), "v"(voff[6]), "v"(voff[7]), "s"(rs), "s"(soff[i]), "s"(wave) \
      : "memory", "scc")
  const auto krs0 = __builtin_amdgcn_make_buffer_rsrc((void*)k, 0, nrec[0], 0x00020000);
  const auto krs1 = __builtin_amdgcn_make_buffer_rsrc((void*)k, 0, nrec[1], 0x00020000);
  const auto vrs0 = __builtin_amdgcn_make_buffer_rsrc((void*)v, 0, nrec[0], 0x00020000);
  const auto vrs1 = __builtin_amdgcn_make_buffer_rsrc((void*)v, 0, nrec[1], 0x00020000);
  LD8("", "", "=&v", K, 0, krs0);
  LD8("", "s_barrier", "=&v", K, 1, krs1);
  LD8("s_cmp_lt_u32 %18, " CFG_EW "\n\ts_cbranch_scc0 1f\n\t", "1:", "=&v", V, 0, vrs0);
  LD8("s_cmp_lt_u32 %18, " CFG_EW "\n\ts_cbranch_scc0 1f\n\t", "1:", "=&v", V, 1, vrs1);
  STAMP(2);
  asm volatile("s_cmp_lt_u32 %18, " CFG_EW "\n\ts_cbranch_scc1 1f\n\ts_waitcnt vmcnt(0)\n\t1:\n\ts_waitcnt vmcnt(16)"
               : "+v"(K[0]), "+v"(K[1]), "+v"(K[2]), "+v"(K[3]), "+v"(K[4]), "+v"(K[5]), "+v"(K[6]), "+v"(K[7]), "+v"(K[8]), "+v"(K[9]),
                 "+v"(K[10]), "+v"(K[11]), "+v"(K[12]), "+v"(K[13]), "+v"(K[14]), "+v"(K[15]), "+v"(qv), "+v"(tag) : "s"(wave) : "scc");
  float bias[NU];
#pragma unroll
  for (int u = 0; u < NU; ++u) {
    unsigned bb = ((part & 1) * 8 + (u & 7) < nl[u >> 3]) ? 0u : 0xff800000u;
    asm volatile("" : "+s"(bb));
    bias[u] = __uint_as_float(bb);
  }
  len = min(max(len, 0), SKV);
  float sc[NU];
  float m = -FLT_MAX;
#pragma unroll
  for (int u = 0; u < NU; ++u) {
    float d = dot2(K[u][0], qv[0], 0.f);
    d = dot2(K[u][1], qv[1], d); d = dot2(K[u][2], qv[2], d); d = dot2(K[u][3], qv[3], d);
    d = rowsum16(d);
    sc[u] = fmaf(d, C2, bias[u]);
    m = fmaxf(m, sc[u]);
  }
  tag = max(tag + 1u, 1u);
  const float msub = m;
  float l = 0.f;
  f32x2 acc[4];
#pragma unroll
  for (int j = 0; j < 4; ++j) acc[j] = f32x2{0.f, 0.f};
#pragma unroll
  for (int u = 0; u < NU; ++u) { sc[u] = __builtin_amdgcn_exp2f(sc[u] - msub); l += sc[u]; }
  if ((lane & 15) == 0)
    __hip_atomic_store(&lds_ml[r * W + wave], ((u64)__float_as_uint(l) << 32) | __float_as_uint(m), __ATOMIC_RELAXED, __HIP_MEMORY_SCOPE_WORKGROUP);
  const auto wrs = __builtin_amdgcn_make_buffer_rsrc((void*)ws, 0, WS_BYTES, 0x00020000);
  unsigned stoff = WS_REC + (unsigned)(((g * RPG + wave * 4 + r) * RPG + rank) * 128 + (lane & 15) * 8);
  unsigned base = WS_REC + (unsigned)(g * RPG + rank) * (RPG * 128) + (unsigned)lane * 16;
  unsigned recoff = (unsigned)(wave * (HG * D) + lane * 8) * 4u;
  const auto ors = __builtin_amdgcn_make_buffer_rsrc((void*)out, 0, H * D * 2, 0x00020000);
  unsigned oob = 0x80000000u;
  unsigned sel1 = (lane & 1) ? 0x03070105u : 0x06020400u, sel2 = (lane & 2) ? 0x03020706u : 0x05040100u;
  float thr = (len < SKV) ? NEGBIG2 + 256.f : -INFINITY;
  unsigned oaddr = (unsigned)((g * HG + part) * D + x * 16 + 2 * (lane & 7) + (lane >> 5)) * 2u + (unsigned)(lane & 24) * 65536u;
  asm volatile("" : "+v"(stoff), "+v"(base), "+v"(tag), "+v"(recoff), "+v"(oob), "+v"(sel1), "+v"(sel2), "+v"(thr), "+v"(oaddr));
  __builtin_amdgcn_sched_barrier(0);
  LD8("s_cmp_lt_u32 %18, " CFG_EW "\n\ts_cbranch_scc1 1f\n\t", "1:", "+v", V, 0, vrs0);
  LD8("s_cmp_lt_u32 %18, " CFG_EW "\n\ts_cbranch_scc1 1f\n\t", "1:", "+v", V, 1, vrs1);
  __builtin_amdgcn_sched_barrier(0);
  float wt = 0.f;
  unsigned nib = 0;
  auto stats = [&](u64 e) {
    const float mw = __uint_as_float((unsigned)e), lw = __uint_as_float((unsigned)(e >> 32));
    float M = fmaxf(mw, dpp<0xB1>(mw));
    M = fmaxf(M, dpp<0x4E>(M));
    M = fmaxf(M, dpp<0x141>(M));
    const float Ms = M;
    float L = __builtin_amdgcn_exp2f(mw - Ms) * lw;
    L += dpp<0xB1>(L); L += dpp<0x4E>(L); L += dpp<0x141>(L);
    wt = __builtin_amdgcn_exp2f(m - Ms);
    const unsigned Mh = __builtin_amdgcn_readlane(__float_as_uint(M), (wave >> 1) * 16);
    const unsigned Lh = __builtin_amdgcn_readlane(__float_as_uint(L), (wave >> 1) * 16);
    nib = ((lane & 8) ? Lh : Mh) >> ((lane & 7) * 4);
  };
  u64* myml = &lds_ml[r * W + (lane & 7)];
  u64 e0 = __hip_atomic_load(myml, __ATOMIC_RELAXED, __HIP_MEMORY_SCOPE_WORKGROUP);
  const bool early = __all((unsigned)(e0 >> 32) != SENT);
  if (early) stats(e0);
  asm volatile("" : "+v"(wt), "+v"(nib));
  STAMP(3);
  __builtin_amdgcn_sched_barrier(0);
#pragma unroll
  for (int u = 0; u < NU; ++u) {
    asm volatile("s_waitcnt vmcnt(%1)" : "+v"(V[u]) : "n"(NU - 1 - u));
#pragma unroll
    for (int j = 0; j < 4; ++j)
      acc[j] = __builtin_elementwise_fma(f32x2{sc[u], sc[u]}, f32x2{__uint_as_float(V[u][j] << 16), __uint_as_float(V[u][j] & 0xffff0000u)}, acc[j]);
    __builtin_amdgcn_sched_barrier(0);
  }
  asm volatile("" : "+v"(acc[0]), "+v"(acc[1]), "+v"(acc[2]), "+v"(acc[3]));
  STAMP(4);
  if (!early) {
    for (;;) {
      e0 = __hip_atomic_load(myml, __ATOMIC_RELAXED, __HIP_MEMORY_SCOPE_WORKGROUP);
      if (__all((unsigned)(e0 >> 32) != SENT)) break;
      __builtin_amdgcn_s_sleep(2);
    }
    stats(e0);
  }
  {
    float* rec = reinterpret_cast<float*>(reinterpret_cast<char*>(lds_acc) + recoff);
    *reinterpret_cast<f32x4*>(rec) = f32x4{acc[0][0] * wt, acc[0][1] * wt, acc[1][0] * wt, acc[1][1] * wt};
    *reinterpret_cast<f32x4*>(rec + 4) = f32x4{acc[2][0] * wt, acc[2][1] * wt, acc[3][0] * wt, acc[3][1] * wt};
  }
  __syncthreads();
  STAMP(5);
  {
    f32x2 a[W / 2];
#pragma unroll
    for (int w = 0; w < W / 2; ++w) a[w] = f32x2{lds_acc[2 * w * (HG * D) + threadIdx.x], lds_acc[(2 * w + 1) * (HG * D) + threadIdx.x]};
    const f32x2 o2 = (a[0] + a[1]) + (a[2] + a[3]);
    const float o = o2[0] + o2[1];
    __builtin_amdgcn_raw_buffer_store_b64(u32x2{(__float_as_uint(o) & ~15u) | (nib & 15u), tag}, wrs, stoff, 0, 16);
  }
  STAMP(6);
  if (wave != 0) { STAMP_FLUSH(); return; }
  unsigned val[8], done;
  float Mr, Lr;
  asm volatile(
      "buffer_load_dwordx4 v[192:195], %[base], %[rs], 0 offen offset:0 sc1\n\t"
      "buffer_load_dwordx4 v[196:199], %[base], %[rs], 0 offen offset:1024 sc1\n\t"
      "buffer_load_dwordx4 v[200:203], %[base], %[rs], 0 offen offset:2048 sc1\n\t"
      "buffer_load_dwordx4 v[204:207], %[base], %[rs], 0 offen offset:3072 sc1\n\t"
      "s_mov_b64 s[64:65], -1\n\t"
      "s_mov_b64 s[66:67], -1\n\t"
      "s_mov_b64 s[68:69], -1\n\t"
      "s_mov_b64 s[70:71], -1\n\t"
      "s_mov_b32 s88, 0x0c0c0400\n\t"
      "s_mov_b32 s89, 0x04000c0c\n\t"
      "s_mov_b32 s90, 0xf0f0f0f0\n\t"
      "s_mov_b32 s91, 0x40000\n\t"
      "s_mov_b32 %[done], 0\n\t"
      "s_sleep 4\n\t"
      "buffer_load_dwordx4 v[208:211], %[base], %[rs], 0 offen offset:0 sc1\n\t"
      "buffer_load_dwordx4 v[212:215], %[base], %[rs], 0 offen offset:1024 sc1\n\t"
      "buffer_load_dwordx4 v[216:219], %[base], %[rs], 0 offen offset:2048 sc1\n\t"
      "buffer_load_dwordx4 v[220:223], %[base], %[rs], 0 offen offset:3072 sc1\n\t"
      "s_sleep 4\n\t"
      "buffer_load_dwordx4 v[224:227], %[base], %[rs], 0 offen offset:0 sc1\n\t"
      "buffer_load_dwordx4 v[228:231], %[base], %[rs], 0 offen offset:1024 sc1\n\t"
      "buffer_load_dwordx4 v[232:235], %[base], %[rs], 0 offen offset:2048 sc1\n\t"
      "buffer_load_dwordx4 v[236:239], %[base], %[rs], 0 offen offset:3072 sc1\n\t"
      "s_sleep 4\n\t"
      "buffer_load_dwordx4 v[240:243], %[base], %[rs], 0 offen offset:0 sc1\n\t"
      "buffer_load_dwordx4 v[244:247], %[base], %[rs], 0 offen offset:1024 sc1\n\t"
      "buffer_load_dwordx4 v[248:251], %[base], %[rs], 0 offen offset:2048 sc1\n\t"
      "buffer_load_dwordx4 v[252:255], %[base], %[rs], 0 offen offset:3072 sc1\n\t"
      "1:\n\t"
      "s_waitcnt vmcnt(12)\n\t"
      "v_cmp_eq_u32_e64 s[72:73], v193, %[tag]\n\t"
      "v_cmp_eq_u32_e64 s[80:81], v195, %[tag]\n\t"
      "v_cmp_eq_u32_e64 s[74:75], v197, %[tag]\n\t"
      "v_cmp_eq_u32_e64 s[82:83], v199, %[tag]\n\t"
      "v_cmp_eq_u32_e64 s[76:77], v201, %[tag]\n\t"
      "v_cmp_eq_u32_e64 s[84:85], v203, %[tag]\n\t"
      "v_cmp_eq_u32_e64 s[78:79], v205, %[tag]\n\t"
      "v_cmp_eq_u32_e64 s[86:87], v207, %[tag]\n\t"
      "s_and_b64 s[72:73], s[72:73], s[80:81]\n\t"
      "s_and_b64 s[74:75], s[74:75], s[82:83]\n\t"
      "s_and_b64 s[76:77], s[76:77], s[84:85]\n\t"
      "s_and_b64 s[78:79], s[78:79], s[86:87]\n\t"
      "v_cndmask_b32_e64 %[x0], %[x0], v192, s[72:73]\n\t"
      "s_andn2_b64 s[64:65], s[64:65], s[72:73]\n\t"
      "v_cndmask_b32_e64 %[x1], %[x1], v194, s[72:73]\n\t"
      "v_cndmask_b32_e64 %[x2], %[x2], v196, s[74:75]\n\t"
      "s_andn2_b64 s[66:67], s[66:67], s[74:75]\n\t"
      "v_cndmask_b32_e64 %[x3], %[x3], v198, s[74:75]\n\t"
      "v_cndmask_b32_e64 %[x4], %[x4], v200, s[76:77]\n\t"
      "s_andn2_b64 s[68:69], s[68:69], s[76:77]\n\t"
      "v_cndmask_b32_e64 %[x5], %[x5], v202, s[76:77]\n\t"
      "v_cndmask_b32_e64 %[x6], %[x6], v204, s[78:79]\n\t"
      "s_andn2_b64 s[70:71], s[70:71], s[78:79]\n\t"
      "v_cndmask_b32_e64 %[x7], %[x7], v206, s[78:79]\n\t"
      "s_or_b64 s[80:81], s[64:65], s[66:67]\n\t"
      "s_or_b64 s[82:83], s[68:69], s[70:71]\n\t"
      "s_or_b64 s[80:81], s[80:81], s[82:83]\n\t"
      "s_cbranch_scc0 2f\n\t"
      "v_cndmask_b32_e64 v188, %[oob], %[base], s[64:65]\n\t"
      "v_cndmask_b32_e64 v189, %[oob], %[base], s[66:67]\n\t"
      "v_cndmask_b32_e64 v190, %[oob], %[base], s[68:69]\n\t"
      "v_cndmask_b32_e64 v191, %[oob], %[base], s[70:71]\n\t"
      "buffer_load_dwordx4 v[192:195], v188, %[rs], 0 offen offset:0 sc1\n\t"
      "buffer_load_dwordx4 v[196:199], v189, %[rs], 0 offen offset:1024 sc1\n\t"
      "buffer_load_dwordx4 v[200:203], v190, %[rs], 0 offen offset:2048 sc1\n\t"
      "buffer_load_dwordx4 v[204:207], v191, %[rs], 0 offen offset:3072 sc1\n\t"
      "s_waitcnt vmcnt(12)\n\t"
      "v_cmp_eq_u32_e64 s[72:73], v209, %[tag]\n\t"
      "v_cmp_eq_u32_e64 s[80:81], v211, %[tag]\n\t"
      "v_cmp_eq_u32_e64 s[74:75], v213, %[tag]\n\t"
      "v_cmp_eq_u32_e64 s[82:83], v215, %[tag]\n\t"
      "v_cmp_eq_u32_e64 s[76:77], v217, %[tag]\n\t"
      "v_cmp_eq_u32_e64 s[84:85], v219, %[tag]\n\t"
      "v_cmp_eq_u32_e64 s[78:79], v221, %[tag]\n\t"
      "v_cmp_eq_u32_e64 s[86:87], v223, %[tag]\n\t"
      "s_and_b64 s[72:73], s[72:73], s[80:81]\n\t"
      "s_and_b64 s[74:75], s[74:75], s[82:83]\n\t"
      "s_and_b64 s[76:77], s[76:77], s[84:85]\n\t"
      "s_and_b64 s[78:79], s[78:79], s[86:87]\n\t"
      "v_cndmask_b32_e64 %[x0], %[x0], v208, s[72:73]\n\t"
      "s_andn2_b64 s[64:65], s[64:65], s[72:73]\n\t"
      "v_cndmask_b32_e64 %[x1], %[x1], v210, s[72:73]\n\t"
      "v_cndmask_b32_e64 %[x2], %[x2], v212, s[74:75]\n\t"
      "s_andn2_b64 s[66:67], s[66:67], s[74:75]\n\t"
      "v_cndmask_b32_e64 %[x3], %[x3], v214, s[74:75]\n\t"
      "v_cndmask_b32_e64 %[x4], %[x4], v216, s[76:77]\n\t"
      "s_andn2_b64 s[68:69], s[68:69], s[76:77]\n\t"
      "v_cndmask_b32_e64 %[x5], %[x5], v218, s[76:77]\n\t"
      "v_cndmask_b32_e64 %[x6], %[x6], v220, s[78:79]\n\t"
      "s_andn2_b64 s[70:71], s[70:71], s[78:79]\n\t"
      "v_cndmask_b32_e64 %[x7], %[x7], v222, s[78:79]\n\t"
      "s_or_b64 s[80:81], s[64:65], s[66:67]\n\t"
      "s_or_b64 s[82:83], s[68:69], s[70:71]\n\t"
      "s_or_b64 s[80:81], s[80:81], s[82:83]\n\t"
      "s_cbranch_scc0 2f\n\t"
      "v_cndmask_b32_e64 v188, %[oob], %[base], s[64:65]\n\t"
      "v_cndmask_b32_e64 v189, %[oob], %[base], s[66:67]\n\t"
      "v_cndmask_b32_e64 v190, %[oob], %[base], s[68:69]\n\t"
      "v_cndmask_b32_e64 v191, %[oob], %[base], s[70:71]\n\t"
      "buffer_load_dwordx4 v[208:211], v188, %[rs], 0 offen offset:0 sc1\n\t"
      "buffer_load_dwordx4 v[212:215], v189, %[rs], 0 offen offset:1024 sc1\n\t"
      "buffer_load_dwordx4 v[216:219], v190, %[rs], 0 offen offset:2048 sc1\n\t"
      "buffer_load_dwordx4 v[220:223], v191, %[rs], 0 offen offset:3072 sc1\n\t"
      "s_waitcnt vmcnt(12)\n\t"
      "v_cmp_eq_u32_e64 s[72:73], v225, %[tag]\n\t"
      "v_cmp_eq_u32_e64 s[80:81], v227, %[tag]\n\t"
      "v_cmp_eq_u32_e64 s[74:75], v229, %[tag]\n\t"
      "v_cmp_eq_u32_e64 s[82:83], v231, %[tag]\n\t"
      "v_cmp_eq_u32_e64 s[76:77], v233, %[tag]\n\t"
      "v_cmp_eq_u32_e64 s[84:85], v235, %[tag]\n\t"
      "v_cmp_eq_u32_e64 s[78:79], v237, %[tag]\n\t"
      "v_cmp_eq_u32_e64 s[86:87], v239, %[tag]\n\t"
      "s_and_b64 s[72:73], s[72:73], s[80:81]\n\t"
      "s_and_b64 s[74:75], s[74:75], s[82:83]\n\t"
      "s_and_b64 s[76:77], s[76:77], s[84:85]\n\t"
      "s_and_b64 s[78:79], s[78:79], s[86:87]\n\t"
      "v_cndmask_b32_e64 %[x0], %[x0], v224, s[72:73]\n\t"
      "s_andn2_b64 s[64:65], s[64:65], s[72:73]\n\t"
      "v_cndmask_b32_e64 %[x1], %[x1], v226, s[72:73]\n\t"
      "v_cndmask_b32_e64 %[x2], %[x2], v228, s[74:75]\n\t"
      "s_andn2_b64 s[66:67], s[66:67], s[74:75]\n\t"
      "v_cndmask_b32_e64 %[x3], %[x3], v230, s[74:75]\n\t"
      "v_cndmask_b32_e64 %[x4], %[x4], v232, s[76:77]\n\t"
      "s_andn2_b64 s[68:69], s[68:69], s[76:77]\n\t"
      "v_cndmask_b32_e64 %[x5], %[x5], v234, s[76:77]\n\t"
      "v_cndmask_b32_e64 %[x6], %[x6], v236, s[78:79]\n\t"
      "s_andn2_b64 s[70:71], s[70:71], s[78:79]\n\t"
      "v_cndmask_b32_e64 %[x7], %[x7], v238, s[78:79]\n\t"
      "s_or_b64 s[80:81], s[64:65], s[66:67]\n\t"
      "s_or_b64 s[82:83], s[68:69], s[70:71]\n\t"
      "s_or_b64 s[80:81], s[80:81], s[82:83]\n\t"
      "s_cbranch_scc0 2f\n\t"
      "v_cndmask_b32_e64 v188, %[oob], %[base], s[64:65]\n\t"
      "v_cndmask_b32_e64 v189, %[oob], %[base], s[66:67]\n\t"
      "v_cndmask_b32_e64 v190, %[oob], %[base], s[68:69]\n\t"
      "v_cndmask_b32_e64 v191, %[oob], %[base], s[70:71]\n\t"
      "buffer_load_dwordx4 v[224:227], v188, %[rs], 0 offen offset:0 sc1\n\t"
      "buffer_load_dwordx4 v[228:231], v189, %[rs], 0 offen offset:1024 sc1\n\t"
      "buffer_load_dwordx4 v[232:235], v190, %[rs], 0 offen offset:2048 sc1\n\t"
      "buffer_load_dwordx4 v[236:239], v191, %[rs], 0 offen offset:3072 sc1\n\t"
      "s_waitcnt vmcnt(12)\n\t"
      "v_cmp_eq_u32_e64 s[72:73], v241, %[tag]\n\t"
      "v_cmp_eq_u32_e64 s[80:81], v243, %[tag]\n\t"
      "v_cmp_eq_u32_e64 s[74:75], v245, %[tag]\n\t"
      "v_cmp_eq_u32_e64 s[82:83], v247, %[tag]\n\t"
      "v_cmp_eq_u32_e64 s[76:77], v249, %[tag]\n\t"
      "v_cmp_eq_u32_e64 s[84:85], v251, %[tag]\n\t"
      "v_cmp_eq_u32_e64 s[78:79], v253, %[tag]\n\t"
      "v_cmp_eq_u32_e64 s[86:87], v255, %[tag]\n\t"
      "s_and_b64 s[72:73], s[72:73], s[80:81]\n\t"
      "s_and_b64 s[74:75], s[74:75], s[82:83]\n\t"
      "s_and_b64 s[76:77], s[76:77], s[84:85]\n\t"
      "s_and_b64 s[78:79], s[78:79], s[86:87]\n\t"
      "v_cndmask_b32_e64 %[x0], %[x0], v240, s[72:73]\n\t"
      "s_andn2_b64 s[64:65], s[64:65], s[72:73]\n\t"
      "v_cndmask_b32_e64 %[x1], %[x1], v242, s[72:73]\n\t"
      "v_cndmask_b32_e64 %[x2], %[x2], v244, s[74:75]\n\t"
      "s_andn2_b64 s[66:67], s[66:67], s[74:75]\n\t"
      "v_cndmask_b32_e64 %[x3], %[x3], v246, s[74:75]\n\t"
      "v_cndmask_b32_e64 %[x4], %[x4], v248, s[76:77]\n\t"
      "s_andn2_b64 s[68:69], s[68:69], s[76:77]\n\t"
      "v_cndmask_b32_e64 %[x5], %[x5], v250, s[76:77]\n\t"
      "v_cndmask_b32_e64 %[x6], %[x6], v252, s[78:79]\n\t"
      "s_andn2_b64 s[70:71], s[70:71], s[78:79]\n\t"
      "v_cndmask_b32_e64 %[x7], %[x7], v254, s[78:79]\n\t"
      "s_or_b64 s[80:81], s[64:65], s[66:67]\n\t"
      "s_or_b64 s[82:83], s[68:69], s[70:71]\n\t"
      "s_or_b64 s[80:81], s[80:81], s[82:83]\n\t"
      "s_cbranch_scc0 2f\n\t"
      "v_cndmask_b32_e64 v188, %[oob], %[base], s[64:65]\n\t"
      "v_cndmask_b32_e64 v189, %[oob], %[base], s[66:67]\n\t"
      "v_cndmask_b32_e64 v190, %[oob], %[base], s[68:69]\n\t"
      "v_cndmask_b32_e64 v191, %[oob], %[base], s[70:71]\n\t"
      "buffer_load_dwordx4 v[240:243], v188, %[rs], 0 offen offset:0 sc1\n\t"
      "buffer_load_dwordx4 v[244:247], v189, %[rs], 0 offen offset:1024 sc1\n\t"
      "buffer_load_dwordx4 v[248:251], v190, %[rs], 0 offen offset:2048 sc1\n\t"
      "buffer_load_dwordx4 v[252:255], v191, %[rs], 0 offen offset:3072 sc1\n\t"
      "s_sub_u32 s91, s91, 1\n\t"
      "s_cbranch_scc0 1b\n\t"
      "2:\n\t"
      "v_perm_b32 v180, %[x2], %[x0], s88\n\t"
      "v_perm_b32 v181, %[x6], %[x4], s89\n\t"
      "v_perm_b32 v182, %[x3], %[x1], s88\n\t"
      "v_perm_b32 v183, %[x7], %[x5], s89\n\t"
      "v_or_b32_e32 v180, v180, v181\n\t"
      "v_or_b32_e32 v182, v182, v183\n\t"
      "v_lshlrev_b32_e32 v182, 4, v182\n\t"
      "v_bfi_b32 v180, s90, v182, v180\n\t"
      "v_and_b32_e32 %[x0], -16, %[x0]\n\t"
      "v_and_b32_e32 %[x1], -16, %[x1]\n\t"
      "v_mov_b32_dpp v181, v180 quad_perm:[1,0,3,2] row_mask:0xf bank_mask:0xf\n\t"
      "v_perm_b32 v180, v181, v180, %[sel1]\n\t"
      "v_and_b32_e32 %[x2], -16, %[x2]\n\t"
      "v_and_b32_e32 %[x3], -16, %[x3]\n\t"
      "v_mov_b32_dpp v181, v180 quad_perm:[2,3,0,1] row_mask:0xf bank_mask:0xf\n\t"
      "v_perm_b32 %[lr], v181, v180, %[sel2]\n\t"
      "v_mov_b32_e32 %[mr], %[lr]\n\t"
      "v_and_b32_e32 %[x4], -16, %[x4]\n\t"
      "v_mov_b32_dpp %[mr], %[lr] row_shr:4 row_mask:0xf bank_mask:0xa\n\t"
      "v_mov_b32_dpp %[lr], %[lr] row_shl:4 row_mask:0xf bank_mask:0x5\n\t"
      "v_and_b32_e32 %[x5], -16, %[x5]\n\t"
      "v_max_f32_dpp v182, %[mr], %[mr] quad_perm:[1,0,3,2] row_mask:0xf bank_mask:0xf\n\t"
      "v_and_b32_e32 %[x6], -16, %[x6]\n\t"
      "v_and_b32_e32 %[x7], -16, %[x7]\n\t"
      "v_max_f32_dpp v182, v182, v182 quad_perm:[2,3,0,1] row_mask:0xf bank_mask:0xf\n\t"
      "s_nop 1\n\t"
      "v_max_f32_dpp v182, v182, v182 row_ror:8 row_mask:0xf bank_mask:0xf\n\t"
      "s_nop 1\n\t"
      "v_max_f32_dpp v182, v182, v182 row_bcast:15 row_mask:0xa bank_mask:0xf\n\t"
      "s_nop 1\n\t"
      "v_max_f32_dpp v182, v182, v182 row_bcast:31 row_mask:0xc bank_mask:0xf\n\t"
      "s_nop 0\n\t"
      "v_readlane_b32 s92, v182, 63\n\t"
      "s_nop 1\n\t"
      "v_subrev_f32_e32 v183, s92, %[mr]\n\t"
      "v_cmp_gt_f32_e32 vcc, s92, %[thr]\n\t"
      "v_exp_f32_e32 v183, v183\n\t"
      "s_cbranch_vccz 3f\n\t"
      "v_mul_f32_e32 v184, v183, %[lr]\n\t"
      "v_mul_f32_dpp v185, v183, %[x0] quad_perm:[0,0,0,0] row_mask:0xf bank_mask:0xf\n\t"
      "v_mul_f32_dpp v186, v183, %[x1] quad_perm:[0,0,0,0] row_mask:0xf bank_mask:0xf\n\t"
      "v_add_f32_dpp v184, v184, v184 quad_perm:[1,0,3,2] row_mask:0xf bank_mask:0xf\n\t"
      "v_fmac_f32_dpp v185, v183, %[x2] quad_perm:[1,1,1,1] row_mask:0xf bank_mask:0xf\n\t"
      "v_fmac_f32_dpp v186, v183, %[x3] quad_perm:[1,1,1,1] row_mask:0xf bank_mask:0xf\n\t"
      "v_add_f32_dpp v184, v184, v184 quad_perm:[2,3,0,1] row_mask:0xf bank_mask:0xf\n\t"
      "v_fmac_f32_dpp v185, v183, %[x4] quad_perm:[2,2,2,2] row_mask:0xf bank_mask:0xf\n\t"
      "v_fmac_f32_dpp v186, v183, %[x5] quad_perm:[2,2,2,2] row_mask:0xf bank_mask:0xf\n\t"
      "v_add_f32_dpp v184, v184, v184 row_ror:8 row_mask:0xf bank_mask:0xf\n\t"
      "v_fmac_f32_dpp v185, v183, %[x6] quad_perm:[3,3,3,3] row_mask:0xf bank_mask:0xf\n\t"
      "v_fmac_f32_dpp v186, v183, %[x7] quad_perm:[3,3,3,3] row_mask:0xf bank_mask:0xf\n\t"
      "v_add_f32_dpp v184, v184, v184 row_bcast:15 row_mask:0xa bank_mask:0xf\n\t"
      "s_nop 0\n\t"
      "v_permlane32_swap_b32_e32 v185, v186\n\t"
      "v_add_f32_dpp v184, v184, v184 row_bcast:31 row_mask:0xc bank_mask:0xf\n\t"
      "v_add_f32_e32 v185, v185, v186\n\t"
      "v_readlane_b32 s93, v184, 63\n\t"
      "s_mov_b32 %[done], 1\n\t"
      "v_add_f32_dpp v185, v185, v185 row_ror:8 row_mask:0xf bank_mask:0xf\n\t"
      "v_rcp_f32_e32 v187, s93\n\t"
      "v_mov_b32_e32 v181, v185\n\t"
      "s_nop 1\n\t"
      "v_permlane16_swap_b32_e32 v185, v181\n\t"
      "v_add_f32_e32 v185, v185, v181\n\t"
      "v_mul_f32_e32 v185, v185, v187\n\t"
      "v_cvt_pk_bf16_f32 v185, v185, v185\n\t"
      "buffer_store_short v185, %[oaddr], %[ors], 0 offen sc1\n\t"
      "3:\n\t"
      "s_nop 1\n\t"
      : [x0] "=&v"(val[0]), [x1] "=&v"(val[1]), [x2] "=&v"(val[2]), [x3] "=&v"(val[3]), [x4] "=&v"(val[4]), [x5] "=&v"(val[5]),
        [x6] "=&v"(val[6]), [x7] "=&v"(val[7]), [mr] "=&v"(Mr), [lr] "=&v"(Lr), [done] "=&s"(done)
      : [base] "v"(base), [oob] "v"(oob), [tag] "v"(tag), [rs] "s"(wrs), [sel1] "v"(sel1), [sel2] "v"(sel2), [thr] "v"(thr),
        [oaddr] "v"(oaddr), [ors] "s"(ors)
      : "memory", "scc", "vcc", "v180", "v181", "v182", "v183", "v184", "v185", "v186", "v187", "v188", "v189", "v190", "v191", "v192", "v193", "v194", "v195", "v196", "v197", "v198", "v199", "v200", "v201", "v202", "v203", "v204", "v205", "v206", "v207", "v208", "v209", "v210", "v211", "v212", "v213", "v214", "v215", "v216", "v217", "v218", "v219", "v220", "v221", "v222", "v223", "v224", "v225", "v226", "v227", "v228", "v229", "v230", "v231", "v232", "v233", "v234", "v235", "v236", "v237", "v238", "v239", "v240", "v241", "v242", "v243", "v244", "v245", "v246", "v247", "v248", "v249", "v250", "v251", "v252", "v253", "v254", "v255", "s64", "s65", "s66", "s67", "s68", "s69", "s70", "s71", "s72", "s73", "s74", "s75", "s76", "s77", "s78", "s79", "s80", "s81", "s82", "s83", "s84", "s85", "s86", "s87", "s88", "s89", "s90", "s91", "s92", "s93");
  STAMP(7);
  if (rank == 0 && lane == 0) __hip_atomic_store((gu32*)(ws + WS_EPOCH + g * 256), tag, __ATOMIC_RELAXED, __HIP_MEMORY_SCOPE_AGENT);
  asm volatile(".Lpf_b:\n\ts_waitcnt vmcnt(0)" ::: "memory");
  if (done) { STAMP_FLUSH(); return; }
  float G = fmaxf(Mr, dpp<0xB1>(Mr));
  G = fmaxf(G, dpp<0x4E>(G));
  G = fmaxf(G, dpp<0x128>(G));
  G = xrow_max(G);
  const float Gs = G;
  const float ws_ = __builtin_amdgcn_exp2f(Mr - Gs);
  float L = ws_ * Lr;
  L += dpp<0xB1>(L); L += dpp<0x4E>(L); L += dpp<0x128>(L); L = xrow_sum(L);
  const float w0 = dpp<0x00>(ws_), w1 = dpp<0x55>(ws_), w2 = dpp<0xAA>(ws_), w3 = dpp<0xFF>(ws_);
#define VAL(i) __uint_as_float(val[i])
  float o0 = fmaf(w1, VAL(2), w0 * VAL(0)) + fmaf(w3, VAL(6), w2 * VAL(4));
  float o1 = fmaf(w1, VAL(3), w0 * VAL(1)) + fmaf(w3, VAL(7), w2 * VAL(5));
  o0 += dpp<0x128>(o0); o1 += dpp<0x128>(o1);
  o0 = xrow_sum(o0); o1 = xrow_sum(o1);
  const int hcol = (g * HG + part) * D + x * 16 + 2 * (lane & 7);
  if (len < SKV) {
    const float Mn = fmaxf(G, NEGBIG2);
    const float a = (L > 0.f) ? __builtin_amdgcn_exp2f(G - Mn) : 0.f;
    const float wm = __builtin_amdgcn_exp2f(NEGBIG2 - Mn);
    float s0 = 0.f, s1 = 0.f;
    for (int j = len; j < SKV; ++j) {
      const unsigned p = (unsigned)bt[j >> 4];
      const unsigned y = *reinterpret_cast<const unsigned*>(v + (size_t)p * (PAGEB / 2) + (size_t)(j & 15) * (TOKB / 2) + hcol);
      s0 += __uint_as_float(y << 16); s1 += __uint_as_float(y & 0xffff0000u);
    }
    L = L * a + wm * (float)(SKV - len); o0 = o0 * a + wm * s0; o1 = o1 * a + wm * s1;
  }
  const float inv = __builtin_amdgcn_rcpf(L);
  if (lane < 8) __hip_atomic_store((gu32*)(out + (hcol >> 1)), pack_bf16(o0 * inv, o1 * inv), __ATOMIC_RELAXED, __HIP_MEMORY_SCOPE_AGENT);
  STAMP_FLUSH();
}

extern "C" void kernel_launch(void* const* d_in, const int* in_sizes, int n_in, void* d_out, int out_size, void* d_ws,
                              size_t ws_size, hipStream_t stream) {
  const unsigned kv_bytes = (unsigned)in_sizes[1] * 2u;
  KArgs a{(const unsigned short*)d_in[0], (const unsigned short*)d_in[1], (const unsigned short*)d_in[2], (const int*)d_in[3],
          (const int*)d_in[4], (unsigned*)d_out, (unsigned char*)d_ws, (unsigned long long)kv_bytes};
  attn_kernel<<<dim3(NWG), dim3(W * 64), 0, stream>>>(a);
}
```

```cpp
#define DIAG
#include <hip/hip_runtime.h>
#include <hip/hip_bf16.h>
#include <stdint.h>
#include <float.h>
#define CFG_PF 0
#define CFG_EW "4"

#define CFG_NT 2

constexpr int H = 32, D = 128, PAGE = 16, NBPS = 256, SKV = NBPS * PAGE;
constexpr int W = 8;
constexpr int NWG = 256;
constexpr int HG = 4;
constexpr int NG = H / HG;
constexpr int RPG = NWG / NG;
constexpr int NU = 16;
constexpr unsigned TOKB = H * D * 2;
constexpr unsigned PAGEB = PAGE * TOKB;
constexpr float C2 = 0.088388347648318440f * 1.4426950408889634f;
constexpr float NEGBIG2 = -1.0e9f * 1.4426950408889634f;

typedef unsigned u32x4 __attribute__((ext_vector_type(4)));
typedef unsigned u32x2 __attribute__((ext_vector_type(2)));
typedef float f32x2 __attribute__((ext_vector_type(2)));
typedef float f32x4 __attribute__((ext_vector_type(4)));
typedef __bf16 bf16x2 __attribute__((ext_vector_type(2)));
#define GAS __attribute__((address_space(1)))
typedef GAS unsigned gu32;

__device__ __forceinline__ float dot2(unsigned a, unsigned b, float c) {
  return __builtin_amdgcn_fdot2_f32_bf16(__builtin_bit_cast(bf16x2, a), __builtin_bit_cast(bf16x2, b), c, false);
}
template <int CTRL> __device__ __forceinline__ float dpp(float x) {
  return __builtin_bit_cast(float, __builtin_amdgcn_mov_dpp(__builtin_bit_cast(int, x), CTRL, 0xf, 0xf, true));
}
__device__ __forceinline__ float rowsum16(float x) {
  x += dpp<0x128>(x); x += dpp<0x124>(x); x += dpp<0x122>(x); x += dpp<0x121>(x); return x;
}
__device__ __forceinline__ float xrow_max(float x) {
  auto s = __builtin_amdgcn_permlane16_swap(__float_as_uint(x), __float_as_uint(x), false, false);
  x = fmaxf(__uint_as_float(s[0]), __uint_as_float(s[1]));
  auto t = __builtin_amdgcn_permlane32_swap(__float_as_uint(x), __float_as_uint(x), false, false);
  return fmaxf(__uint_as_float(t[0]), __uint_as_float(t[1]));
}
__device__ __forceinline__ float xrow_sum(float x) {
  auto s = __builtin_amdgcn_permlane16_swap(__float_as_uint(x), __float_as_uint(x), false, false);
  x = __uint_as_float(s[0]) + __uint_as_float(s[1]);
  auto t = __builtin_amdgcn_permlane32_swap(__float_as_uint(x), __float_as_uint(x), false, false);
  return __uint_as_float(t[0]) + __uint_as_float(t[1]);
}
__device__ __forceinline__ unsigned pack_bf16(float a, float b) {
  bf16x2 r; r[0] = (__bf16)a; r[1] = (__bf16)b; return __builtin_bit_cast(unsigned, r);
}

constexpr unsigned WS_EPOCH = 0, WS_REC = 4096;
constexpr unsigned WS_BYTES = WS_REC + NG * RPG * RPG * 128;
constexpr unsigned SENT = 0xBF800000u;

#define DIAG_N 9
#define DIAG_WAVES (NWG * W)
#define DIAG_BLOCKS (NWG)
#define DIAG_OFF (64u << 20)
#define STAMP(i) do { __builtin_amdgcn_sched_barrier(0); unsigned long long t_; asm volatile("s_memrealtime %0\n\ts_waitcnt lgkmcnt(0)" : "=s"(t_) :: "memory"); st_[i] = t_; __builtin_amdgcn_sched_barrier(0); } while (0)
#define STAMP_FLUSH() do { if ((threadIdx.x & 63) == 0) { unsigned long long* p_ = (unsigned long long*)(ws + DIAG_OFF) + (size_t)(blockIdx.x * W + (threadIdx.x >> 6)) * DIAG_N; _Pragma("unroll") for (int i_ = 0; i_ < DIAG_N; ++i_) p_[i_] = st_[i_]; } } while (0)
#define KEEPV(x) asm volatile("" :: "v"(x))

struct KArgs { const unsigned short* q; const unsigned short* k; const unsigned short* v; const int* bt; const int* lens; unsigned* out; unsigned char* ws; unsigned long long kv_bytes; };
typedef __attribute__((address_space(4))) const KArgs* KArgsP;
typedef __attribute__((address_space(4))) const int* CIntP;
typedef unsigned long long u64;

__global__ void __launch_bounds__(W * 64, W / 4) attn_kernel(KArgs unused_args) {
  __shared__ __attribute__((aligned(16))) float lds_acc[W * HG * D];
  __shared__ __attribute__((aligned(16))) u64 lds_ml[HG * W];
  unsigned long long st_[DIAG_N];
#pragma unroll
  for (int i = 0; i < DIAG_N; ++i) st_[i] = 0;
  STAMP(0);
  st_[8] = ((unsigned long long)((unsigned)__builtin_amdgcn_s_getreg(20 | (3 << 11)) & 0xfu) << 32) | (unsigned)__builtin_amdgcn_s_getreg(63492);
  KArgsP ka = (KArgsP)__builtin_amdgcn_kernarg_segment_ptr();
  asm volatile("" : "+s"(ka));
  const unsigned short* q = ka->q; const unsigned short* k = ka->k; const unsigned short* v = ka->v;
  CIntP bt = (CIntP)ka->bt; CIntP lens = (CIntP)ka->lens; unsigned* out = ka->out; unsigned char* ws = ka->ws;
  unsigned long long kvb = ka->kv_bytes;

  const int x = blockIdx.x & 7, g = (blockIdx.x >> 3) & 7, part = blockIdx.x >> 6;
  const int rank = part * 8 + x;
  const int lane = threadIdx.x & 63, wave = __builtin_amdgcn_readfirstlane(threadIdx.x >> 6);
  const int r = lane >> 4;

  if (threadIdx.x < HG * W) __hip_atomic_store(&lds_ml[threadIdx.x], (u64)SENT << 32, __ATOMIC_RELAXED, __HIP_MEMORY_SCOPE_WORKGROUP);
  unsigned lpb = (unsigned)((wave * 2 + (part >> 1)) * 8 + x) * 4u;
  asm volatile("" : "+s"(lpb));
  asm volatile("s_waitcnt lgkmcnt(0)" : "+s"(bt), "+s"(lens), "+s"(kvb) :: "memory");
  const unsigned kv_bytes = (unsigned)kvb;
  int len = lens[0];
  int lp[2], pg[2];
#pragma unroll
  for (int i = 0; i < 2; ++i) { lp[i] = (int)(lpb >> 2) + i * 128; pg[i] = *(CIntP)((const __attribute__((address_space(4))) char*)bt + lpb + i * 512); }
  u32x4 qv;
  unsigned tag;
  asm volatile("global_load_dwordx4 %0, %2, %3\n\tglobal_load_dword %1, %4, %5 sc1"
               : "=&v"(qv), "=&v"(tag) : "v"((unsigned)(g * (HG * D * 2) + lane * 16)), "s"(q), "v"((unsigned)(WS_EPOCH + g * 256)), "s"(ws) : "memory");
  unsigned voff[8];
#pragma unroll
  for (int t = 0; t < 8; ++t) {
    voff[t] = (unsigned)g * (HG * D * 2) + (unsigned)lane * 16 + (unsigned)((part & 1) * 8 + t) * TOKB;
    asm volatile("" : "+v"(voff[t]));
  }
  __builtin_amdgcn_sched_barrier(0);
  __builtin_amdgcn_s_barrier();
  __builtin_amdgcn_sched_barrier(0);
  int nl[2];
  unsigned soff[2], nrec[2];
#pragma unroll
  for (int i = 0; i < 2; ++i) {
    int n = max(len, lp[i] * PAGE) - lp[i] * PAGE;
    asm volatile("" : "+s"(n));
    nl[i] = min(n, PAGE);
    soff[i] = (unsigned)pg[i] * PAGEB;
    nrec[i] = min(kv_bytes, soff[i] + (unsigned)nl[i] * TOKB);
  }
  u32x4 K[NU], V[NU];
  STAMP(1);
#define LD8(pre, post, OUTC, X, i, rs) asm volatile(pre \
      "buffer_load_dwordx4 %0, %8, %16, %17 offen nt\n\tbuffer_load_dwordx4 %1, %9, %16, %17 offen nt\n\t" \
      "buffer_load_dwordx4 %2, %10, %16, %17 offen nt\n\tbuffer_load_dwordx4 %3, %11, %16, %17 offen nt\n\t" \
      "buffer_load_dwordx4 %4, %12, %16, %17 offen nt\n\tbuffer_load_dwordx4 %5, %13, %16, %17 offen nt\n\t" \
      "buffer_load_dwordx4 %6, %14, %16, %17 offen nt\n\tbuffer_load_dwordx4 %7, %15, %16, %17 offen nt\n\t" post \
      : OUTC(X[i * 8 + 0]), OUTC(X[i * 8 + 1]), OUTC(X[i * 8 + 2]), OUTC(X[i * 8 + 3]), OUTC(X[i * 8 + 4]), OUTC(X[i * 8 + 5]), OUTC(X[i * 8 + 6]), OUTC(X[i * 8 + 7]) \
      : "v"(voff[0]), "v"(voff[1]), "v"(voff[2]), "v"(voff[3]), "v"(voff[4]), "v"(voff[5]), "v"(voff[6]), "v"(voff[7]), "s"(rs), "s"(soff[i]), "s"(wave) \
      : "memory", "scc")
  const auto krs0 = __builtin_amdgcn_make_buffer_rsrc((void*)k, 0, nrec[0], 0x00020000);
  const auto krs1 = __builtin_amdgcn_make_buffer_rsrc((void*)k, 0, nrec[1], 0x00020000);
  const auto vrs0 = __builtin_amdgcn_make_buffer_rsrc((void*)v, 0, nrec[0], 0x00020000);
  const auto vrs1 = __builtin_amdgcn_make_buffer_rsrc((void*)v, 0, nrec[1], 0x00020000);
  LD8("", "", "=&v", K, 0, krs0);
  LD8("", "s_barrier", "=&v", K, 1, krs1);
  LD8("s_cmp_lt_u32 %18, " CFG_EW "\n\ts_cbranch_scc0 1f\n\t", "1:", "=&v", V, 0, vrs0);
  LD8("s_cmp_lt_u32 %18, " CFG_EW "\n\ts_cbranch_scc0 1f\n\t", "1:", "=&v", V, 1, vrs1);
  STAMP(2);
  asm volatile("s_cmp_lt_u32 %18, " CFG_EW "\n\ts_cbranch_scc1 1f\n\ts_waitcnt vmcnt(0)\n\t1:\n\ts_waitcnt vmcnt(16)"
               : "+v"(K[0]), "+v"(K[1]), "+v"(K[2]), "+v"(K[3]), "+v"(K[4]), "+v"(K[5]), "+v"(K[6]), "+v"(K[7]), "+v"(K[8]), "+v"(K[9]),
                 "+v"(K[10]), "+v"(K[11]), "+v"(K[12]), "+v"(K[13]), "+v"(K[14]), "+v"(K[15]), "+v"(qv), "+v"(tag) : "s"(wave) : "scc");
  float bias[NU];
#pragma unroll
  for (int u = 0; u < NU; ++u) {
    unsigned bb = ((part & 1) * 8 + (u & 7) < nl[u >> 3]) ? 0u : 0xff800000u;
    asm volatile("" : "+s"(bb));
    bias[u] = __uint_as_float(bb);
  }
  len = min(max(len, 0), SKV);
  float sc[NU];
  float m = -FLT_MAX;
#pragma unroll
  for (int u = 0; u < NU; ++u) {
    float d = dot2(K[u][0], qv[0], 0.f);
    d = dot2(K[u][1], qv[1], d); d = dot2(K[u][2], qv[2], d); d = dot2(K[u][3], qv[3], d);
    d = rowsum16(d);
    sc[u] = fmaf(d, C2, bias[u]);
    m = fmaxf(m, sc[u]);
  }
  tag = max(tag + 1u, 1u);
  const float msub = m;
  float l = 0.f;
  f32x2 acc[4];
#pragma unroll
  for (int j = 0; j < 4; ++j) acc[j] = f32x2{0.f, 0.f};
#pragma unroll
  for (int u = 0; u < NU; ++u) { sc[u] = __builtin_amdgcn_exp2f(sc[u] - msub); l += sc[u]; }
  if ((lane & 15) == 0)
    __hip_atomic_store(&lds_ml[r * W + wave], ((u64)__float_as_uint(l) << 32) | __float_as_uint(m), __ATOMIC_RELAXED, __HIP_MEMORY_SCOPE_WORKGROUP);
  const auto wrs = __builtin_amdgcn_make_buffer_rsrc((void*)ws, 0, WS_BYTES, 0x00020000);
  unsigned stoff = WS_REC + (unsigned)(((g * RPG + wave * 4 + r) * RPG + rank) * 128 + (lane & 15) * 8);
  unsigned base = WS_REC + (unsigned)(g * RPG + rank) * (RPG * 128) + (unsigned)lane * 16;
  unsigned recoff = (unsigned)(wave * (HG * D) + lane * 8) * 4u;
  const auto ors = __builtin_amdgcn_make_buffer_rsrc((void*)out, 0, H * D * 2, 0x00020000);
  unsigned oob = 0x80000000u;
  unsigned sel1 = (lane & 1) ? 0x03070105u : 0x06020400u, sel2 = (lane & 2) ? 0x03020706u : 0x05040100u;
  float thr = (len < SKV) ? NEGBIG2 + 256.f : -INFINITY;
  unsigned oaddr = (unsigned)((g * HG + part) * D + x * 16 + 2 * (lane & 7) + (lane >> 5)) * 2u + (unsigned)(lane & 24) * 65536u;
  asm volatile("" : "+v"(stoff), "+v"(base), "+v"(tag), "+v"(recoff), "+v"(oob), "+v"(sel1), "+v"(sel2), "+v"(thr), "+v"(oaddr));
  __builtin_amdgcn_sched_barrier(0);
  LD8("s_cmp_lt_u32 %18, " CFG_EW "\n\ts_cbranch_scc1 1f\n\t", "1:", "+v", V, 0, vrs0);
  LD8("s_cmp_lt_u32 %18, " CFG_EW "\n\ts_cbranch_scc1 1f\n\t", "1:", "+v", V, 1, vrs1);
  __builtin_amdgcn_sched_barrier(0);
  float wt = 0.f;
  unsigned nib = 0;
  auto stats = [&](u64 e) {
    const float mw = __uint_as_float((unsigned)e), lw = __uint_as_float((unsigned)(e >> 32));
    float M = fmaxf(mw, dpp<0xB1>(mw));
    M = fmaxf(M, dpp<0x4E>(M));
    M = fmaxf(M, dpp<0x141>(M));
    const float Ms = M;
    float L = __builtin_amdgcn_exp2f(mw - Ms) * lw;
    L += dpp<0xB1>(L); L += dpp<0x4E>(L); L += dpp<0x141>(L);
    wt = __builtin_amdgcn_exp2f(m - Ms);
    const unsigned Mh = __builtin_amdgcn_readlane(__float_as_uint(M), (wave >> 1) * 16);
    const unsigned Lh = __builtin_amdgcn_readlane(__float_as_uint(L), (wave >> 1) * 16);
    nib = ((lane & 8) ? Lh : Mh) >> ((lane & 7) * 4);
  };
  u64* myml = &lds_ml[r * W + (lane & 7)];
  u64 e0 = __hip_atomic_load(myml, __ATOMIC_RELAXED, __HIP_MEMORY_SCOPE_WORKGROUP);
  const bool early = __all((unsigned)(e0 >> 32) != SENT);
  if (early) stats(e0);
  asm volatile("" : "+v"(wt), "+v"(nib));
  STAMP(3);
  __builtin_amdgcn_sched_barrier(0);
#pragma unroll
  for (int u = 0; u < NU; ++u) {
    asm volatile("s_waitcnt vmcnt(%1)" : "+v"(V[u]) : "n"(NU - 1 - u));
#pragma unroll
    for (int j = 0; j < 4; ++j)
      acc[j] = __builtin_elementwise_fma(f32x2{sc[u], sc[u]}, f32x2{__uint_as_float(V[u][j] << 16), __uint_as_float(V[u][j] & 0xffff0000u)}, acc[j]);
    __builtin_amdgcn_sched_barrier(0);
  }
  asm volatile("" : "+v"(acc[0]), "+v"(acc[1]), "+v"(acc[2]), "+v"(acc[3]));
  STAMP(4);
  if (!early) {
    for (;;) {
      e0 = __hip_atomic_load(myml, __ATOMIC_RELAXED, __HIP_MEMORY_SCOPE_WORKGROUP);
      if (__all((unsigned)(e0 >> 32) != SENT)) break;
      __builtin_amdgcn_s_sleep(2);
    }
    stats(e0);
  }
  {
    float* rec = reinterpret_cast<float*>(reinterpret_cast<char*>(lds_acc) + recoff);
    *reinterpret_cast<f32x4*>(rec) = f32x4{acc[0][0] * wt, acc[0][1] * wt, acc[1][0] * wt, acc[1][1] * wt};
    *reinterpret_cast<f32x4*>(rec + 4) = f32x4{acc[2][0] * wt, acc[2][1] * wt, acc[3][0] * wt, acc[3][1] * wt};
  }
  __syncthreads();
  STAMP(5);
  {
    f32x2 a[W / 2];
#pragma unroll
    for (int w = 0; w < W / 2; ++w) a[w] = f32x2{lds_acc[2 * w * (HG * D) + threadIdx.x], lds_acc[(2 * w + 1) * (HG * D) + threadIdx.x]};
    const f32x2 o2 = (a[0] + a[1]) + (a[2] + a[3]);
    const float o = o2[0] + o2[1];
    __builtin_amdgcn_raw_buffer_store_b64(u32x2{(__float_as_uint(o) & ~15u) | (nib & 15u), tag}, wrs, stoff, 0, 16);
  }
  STAMP(6);
  if (wave != 0) { STAMP_FLUSH(); return; }
  unsigned val[8], done;
  float Mr, Lr;
  asm volatile(
      "buffer_load_dwordx4 v[192:195], %[base], %[rs], 0 offen offset:0 sc1\n\t"
      "buffer_load_dwordx4 v[196:199], %[base], %[rs], 0 offen offset:1024 sc1\n\t"
      "buffer_load_dwordx4 v[200:203], %[base], %[rs], 0 offen offset:2048 sc1\n\t"
      "buffer_load_dwordx4 v[204:207], %[base], %[rs], 0 offen offset:3072 sc1\n\t"
      "s_mov_b64 s[64:65], -1\n\t"
      "s_mov_b64 s[66:67], -1\n\t"
      "s_mov_b64 s[68:69], -1\n\t"
      "s_mov_b64 s[70:71], -1\n\t"
      "s_mov_b32 s88, 0x0c0c0400\n\t"
      "s_mov_b32 s89, 0x04000c0c\n\t"
      "s_mov_b32 s90, 0xf0f0f0f0\n\t"
      "s_mov_b32 s91, 0x40000\n\t"
      "s_mov_b32 %[done], 0\n\t"
      "s_sleep 4\n\t"
      "buffer_load_dwordx4 v[208:211], %[base], %[rs], 0 offen offset:0 sc1\n\t"
      "buffer_load_dwordx4 v[212:215], %[base], %[rs], 0 offen offset:1024 sc1\n\t"
      "buffer_load_dwordx4 v[216:219], %[base], %[rs], 0 offen offset:2048 sc1\n\t"
      "buffer_load_dwordx4 v[220:223], %[base], %[rs], 0 offen offset:3072 sc1\n\t"
      "s_sleep 4\n\t"
      "buffer_load_dwordx4 v[224:227], %[base], %[rs], 0 offen offset:0 sc1\n\t"
      "buffer_load_dwordx4 v[228:231], %[base], %[rs], 0 offen offset:1024 sc1\n\t"
      "buffer_load_dwordx4 v[232:235], %[base], %[rs], 0 offen offset:2048 sc1\n\t"
      "buffer_load_dwordx4 v[236:239], %[base], %[rs], 0 offen offset:3072 sc1\n\t"
      "s_sleep 4\n\t"
      "buffer_load_dwordx4 v[240:243], %[base], %[rs], 0 offen offset:0 sc1\n\t"
      "buffer_load_dwordx4 v[244:247], %[base], %[rs], 0 offen offset:1024 sc1\n\t"
      "buffer_load_dwordx4 v[248:251], %[base], %[rs], 0 offen offset:2048 sc1\n\t"
      "buffer_load_dwordx4 v[252:255], %[base], %[rs], 0 offen offset:3072 sc1\n\t"
      "1:\n\t"
      "s_waitcnt vmcnt(12)\n\t"
      "v_cmp_eq_u32_e64 s[72:73], v193, %[tag]\n\t"
      "v_cmp_eq_u32_e64 s[80:81], v195, %[tag]\n\t"
      "v_cmp_eq_u32_e64 s[74:75], v197, %[tag]\n\t"
      "v_cmp_eq_u32_e64 s[82:83], v199, %[tag]\n\t"
      "v_cmp_eq_u32_e64 s[76:77], v201, %[tag]\n\t"
      "v_cmp_eq_u32_e64 s[84:85], v203, %[tag]\n\t"
      "v_cmp_eq_u32_e64 s[78:79], v205, %[tag]\n\t"
      "v_cmp_eq_u32_e64 s[86:87], v207, %[tag]\n\t"
      "s_and_b64 s[72:73], s[72:73], s[80:81]\n\t"
      "s_and_b64 s[74:75], s[74:75], s[82:83]\n\t"
      "s_and_b64 s[76:77], s[76:77], s[84:85]\n\t"
      "s_and_b64 s[78:79], s[78:79], s[86:87]\n\t"
      "v_cndmask_b32_e64 %[x0], %[x0], v192, s[72:73]\n\t"
      "s_andn2_b64 s[64:65], s[64:65], s[72:73]\n\t"
      "v_cndmask_b32_e64 %[x1], %[x1], v194, s[72:73]\n\t"
      "v_cndmask_b32_e64 %[x2], %[x2], v196, s[74:75]\n\t"
      "s_andn2_b64 s[66:67], s[66:67], s[74:75]\n\t"
      "v_cndmask_b32_e64 %[x3], %[x3], v198, s[74:75]\n\t"
      "v_cndmask_b32_e64 %[x4], %[x4], v200, s[76:77]\n\t"
      "s_andn2_b64 s[68:69], s[68:69], s[76:77]\n\t"
      "v_cndmask_b32_e64 %[x5], %[x5], v202, s[76:77]\n\t"
      "v_cndmask_b32_e64 %[x6], %[x6], v204, s[78:79]\n\t"
      "s_andn2_b64 s[70:71], s[70:71], s[78:79]\n\t"
      "v_cndmask_b32_e64 %[x7], %[x7], v206, s[78:79]\n\t"
      "s_or_b64 s[80:81], s[64:65], s[66:67]\n\t"
      "s_or_b64 s[82:83], s[68:69], s[70:71]\n\t"
      "s_or_b64 s[80:81], s[80:81], s[82:83]\n\t"
      "s_cbranch_scc0 2f\n\t"
      "v_cndmask_b32_e64 v188, %[oob], %[base], s[64:65]\n\t"
      "v_cndmask_b32_e64 v189, %[oob], %[base], s[66:67]\n\t"
      "v_cndmask_b32_e64 v190, %[oob], %[base], s[68:69]\n\t"
      "v_cndmask_b32_e64 v191, %[oob], %[base], s[70:71]\n\t"
      "buffer_load_dwordx4 v[192:195], v188, %[rs], 0 offen offset:0 sc1\n\t"
      "buffer_load_dwordx4 v[196:199], v189, %[rs], 0 offen offset:1024 sc1\n\t"
      "buffer_load_dwordx4 v[200:203], v190, %[rs], 0 offen offset:2048 sc1\n\t"
      "buffer_load_dwordx4 v[204:207], v191, %[rs], 0 offen offset:3072 sc1\n\t"
      "s_waitcnt vmcnt(12)\n\t"
      "v_cmp_eq_u32_e64 s[72:73], v209, %[tag]\n\t"
      "v_cmp_eq_u32_e64 s[80:81], v211, %[tag]\n\t"
      "v_cmp_eq_u32_e64 s[74:75], v213, %[tag]\n\t"
      "v_cmp_eq_u32_e64 s[82:83], v215, %[tag]\n\t"
      "v_cmp_eq_u32_e64 s[76:77], v217, %[tag]\n\t"
      "v_cmp_eq_u32_e64 s[84:85], v219, %[tag]\n\t"
      "v_cmp_eq_u32_e64 s[78:79], v221, %[tag]\n\t"
      "v_cmp_eq_u32_e64 s[86:87], v223, %[tag]\n\t"
      "s_and_b64 s[72:73], s[72:73], s[80:81]\n\t"
      "s_and_b64 s[74:75], s[74:75], s[82:83]\n\t"
      "s_and_b64 s[76:77], s[76:77], s[84:85]\n\t"
      "s_and_b64 s[78:79], s[78:79], s[86:87]\n\t"
      "v_cndmask_b32_e64 %[x0], %[x0], v208, s[72:73]\n\t"
      "s_andn2_b64 s[64:65], s[64:65], s[72:73]\n\t"
      "v_cndmask_b32_e64 %[x1], %[x1], v210, s[72:73]\n\t"
      "v_cndmask_b32_e64 %[x2], %[x2], v212, s[74:75]\n\t"
      "s_andn2_b64 s[66:67], s[66:67], s[74:75]\n\t"
      "v_cndmask_b32_e64 %[x3], %[x3], v214, s[74:75]\n\t"
      "v_cndmask_b32_e64 %[x4], %[x4], v216, s[76:77]\n\t"
      "s_andn2_b64 s[68:69], s[68:69], s[76:77]\n\t"
      "v_cndmask_b32_e64 %[x5], %[x5], v218, s[76:77]\n\t"
      "v_cndmask_b32_e64 %[x6], %[x6], v220, s[78:79]\n\t"
      "s_andn2_b64 s[70:71], s[70:71], s[78:79]\n\t"
      "v_cndmask_b32_e64 %[x7], %[x7], v222, s[78:79]\n\t"
      "s_or_b64 s[80:81], s[64:65], s[66:67]\n\t"
      "s_or_b64 s[82:83], s[68:69], s[70:71]\n\t"
      "s_or_b64 s[80:81], s[80:81], s[82:83]\n\t"
      "s_cbranch_scc0 2f\n\t"
      "v_cndmask_b32_e64 v188, %[oob], %[base], s[64:65]\n\t"
      "v_cndmask_b32_e64 v189, %[oob], %[base], s[66:67]\n\t"
      "v_cndmask_b32_e64 v190, %[oob], %[base], s[68:69]\n\t"
      "v_cndmask_b32_e64 v191, %[oob], %[base], s[70:71]\n\t"
      "buffer_load_dwordx4 v[208:211], v188, %[rs], 0 offen offset:0 sc1\n\t"
      "buffer_load_dwordx4 v[212:215], v189, %[rs], 0 offen offset:1024 sc1\n\t"
      "buffer_load_dwordx4 v[216:219], v190, %[rs], 0 offen offset:2048 sc1\n\t"
      "buffer_load_dwordx4 v[220:223], v191, %[rs], 0 offen offset:3072 sc1\n\t"
      "s_waitcnt vmcnt(12)\n\t"
      "v_cmp_eq_u32_e64 s[72:73], v225, %[tag]\n\t"
      "v_cmp_eq_u32_e64 s[80:81], v227, %[tag]\n\t"
      "v_cmp_eq_u32_e64 s[74:75], v229, %[tag]\n\t"
      "v_cmp_eq_u32_e64 s[82:83], v231, %[tag]\n\t"
      "v_cmp_eq_u32_e64 s[76:77], v233, %[tag]\n\t"
      "v_cmp_eq_u32_e64 s[84:85], v235, %[tag]\n\t"
      "v_cmp_eq_u32_e64 s[78:79], v237, %[tag]\n\t"
      "v_cmp_eq_u32_e64 s[86:87], v239, %[tag]\n\t"
      "s_and_b64 s[72:73], s[72:73], s[80:81]\n\t"
      "s_and_b64 s[74:75], s[74:75], s[82:83]\n\t"
      "s_and_b64 s[76:77], s[76:77], s[84:85]\n\t"
      "s_and_b64 s[78:79], s[78:79], s[86:87]\n\t"
      "v_cndmask_b32_e64 %[x0], %[x0], v224, s[72:73]\n\t"
      "s_andn2_b64 s[64:65], s[64:65], s[72:73]\n\t"
      "v_cndmask_b32_e64 %[x1], %[x1], v226, s[72:73]\n\t"
      "v_cndmask_b32_e64 %[x2], %[x2], v228, s[74:75]\n\t"
      "s_andn2_b64 s[66:67], s[66:67], s[74:75]\n\t"
      "v_cndmask_b32_e64 %[x3], %[x3], v230, s[74:75]\n\t"
      "v_cndmask_b32_e64 %[x4], %[x4], v232, s[76:77]\n\t"
      "s_andn2_b64 s[68:69], s[68:69], s[76:77]\n\t"
      "v_cndmask_b32_e64 %[x5], %[x5], v234, s[76:77]\n\t"
      "v_cndmask_b32_e64 %[x6], %[x6], v236, s[78:79]\n\t"
      "s_andn2_b64 s[70:71], s[70:71], s[78:79]\n\t"
      "v_cndmask_b32_e64 %[x7], %[x7], v238, s[78:79]\n\t"
      "s_or_b64 s[80:81], s[64:65], s[66:67]\n\t"
      "s_or_b64 s[82:83], s[68:69], s[70:71]\n\t"
      "s_or_b64 s[80:81], s[80:81], s[82:83]\n\t"
      "s_cbranch_scc0 2f\n\t"
      "v_cndmask_b32_e64 v188, %[oob], %[base], s[64:65]\n\t"
      "v_cndmask_b32_e64 v189, %[oob], %[base], s[66:67]\n\t"
      "v_cndmask_b32_e64 v190, %[oob], %[base], s[68:69]\n\t"
      "v_cndmask_b32_e64 v191, %[oob], %[base], s[70:71]\n\t"
      "buffer_load_dwordx4 v[224:227], v188, %[rs], 0 offen offset:0 sc1\n\t"
      "buffer_load_dwordx4 v[228:231], v189, %[rs], 0 offen offset:1024 sc1\n\t"
      "buffer_load_dwordx4 v[232:235], v190, %[rs], 0 offen offset:2048 sc1\n\t"
      "buffer_load_dwordx4 v[236:239], v191, %[rs], 0 offen offset:3072 sc1\n\t"
      "s_waitcnt vmcnt(12)\n\t"
      "v_cmp_eq_u32_e64 s[72:73], v241, %[tag]\n\t"
      "v_cmp_eq_u32_e64 s[80:81], v243, %[tag]\n\t"
      "v_cmp_eq_u32_e64 s[74:75], v245, %[tag]\n\t"
      "v_cmp_eq_u32_e64 s[82:83], v247, %[tag]\n\t"
      "v_cmp_eq_u32_e64 s[76:77], v249, %[tag]\n\t"
      "v_cmp_eq_u32_e64 s[84:85], v251, %[tag]\n\t"
      "v_cmp_eq_u32_e64 s[78:79], v253, %[tag]\n\t"
      "v_cmp_eq_u32_e64 s[86:87], v255, %[tag]\n\t"
      "s_and_b64 s[72:73], s[72:73], s[80:81]\n\t"
      "s_and_b64 s[74:75], s[74:75], s[82:83]\n\t"
      "s_and_b64 s[76:77], s[76:77], s[84:85]\n\t"
      "s_and_b64 s[78:79], s[78:79], s[86:87]\n\t"
      "v_cndmask_b32_e64 %[x0], %[x0], v240, s[72:73]\n\t"
      "s_andn2_b64 s[64:65], s[64:65], s[72:73]\n\t"
      "v_cndmask_b32_e64 %[x1], %[x1], v242, s[72:73]\n\t"
      "v_cndmask_b32_e64 %[x2], %[x2], v244, s[74:75]\n\t"
      "s_andn2_b64 s[66:67], s[66:67], s[74:75]\n\t"
      "v_cndmask_b32_e64 %[x3], %[x3], v246, s[74:75]\n\t"
      "v_cndmask_b32_e64 %[x4], %[x4], v248, s[76:77]\n\t"
      "s_andn2_b64 s[68:69], s[68:69], s[76:77]\n\t"
      "v_cndmask_b32_e64 %[x5], %[x5], v250, s[76:77]\n\t"
      "v_cndmask_b32_e64 %[x6], %[x6], v252, s[78:79]\n\t"
      "s_andn2_b64 s[70:71], s[70:71], s[78:79]\n\t"
      "v_cndmask_b32_e64 %[x7], %[x7], v254, s[78:79]\n\t"
      "s_or_b64 s[80:81], s[64:65], s[66:67]\n\t"
      "s_or_b64 s[82:83], s[68:69], s[70:71]\n\t"
      "s_or_b64 s[80:81], s[80:81], s[82:83]\n\t"
      "s_cbranch_scc0 2f\n\t"
      "v_cndmask_b32_e64 v188, %[oob], %[base], s[64:65]\n\t"
      "v_cndmask_b32_e64 v189, %[oob], %[base], s[66:67]\n\t"
      "v_cndmask_b32_e64 v190, %[oob], %[base], s[68:69]\n\t"
      "v_cndmask_b32_e64 v191, %[oob], %[base], s[70:71]\n\t"
      "buffer_load_dwordx4 v[240:243], v188, %[rs], 0 offen offset:0 sc1\n\t"
      "buffer_load_dwordx4 v[244:247], v189, %[rs], 0 offen offset:1024 sc1\n\t"
      "buffer_load_dwordx4 v[248:251], v190, %[rs], 0 offen offset:2048 sc1\n\t"
      "buffer_load_dwordx4 v[252:255], v191, %[rs], 0 offen offset:3072 sc1\n\t"
      "s_sub_u32 s91, s91, 1\n\t"
      "s_cbranch_scc0 1b\n\t"
      "2:\n\t"
      "v_perm_b32 v180, %[x2], %[x0], s88\n\t"
      "v_perm_b32 v181, %[x6], %[x4], s89\n\t"
      "v_perm_b32 v182, %[x3], %[x1], s88\n\t"
      "v_perm_b32 v183, %[x7], %[x5], s89\n\t"
      "v_or_b32_e32 v180, v180, v181\n\t"
      "v_or_b32_e32 v182, v182, v183\n\t"
      "v_lshlrev_b32_e32 v182, 4, v182\n\t"
      "v_bfi_b32 v180, s90, v182, v180\n\t"
      "v_and_b32_e32 %[x0], -16, %[x0]\n\t"
      "v_and_b32_e32 %[x1], -16, %[x1]\n\t"
      "v_mov_b32_dpp v181, v180 quad_perm:[1,0,3,2] row_mask:0xf bank_mask:0xf\n\t"
      "v_perm_b32 v180, v181, v180, %[sel1]\n\t"
      "v_and_b32_e32 %[x2], -16, %[x2]\n\t"
      "v_and_b32_e32 %[x3], -16, %[x3]\n\t"
      "v_mov_b32_dpp v181, v180 quad_perm:[2,3,0,1] row_mask:0xf bank_mask:0xf\n\t"
      "v_perm_b32 %[lr], v181, v180, %[sel2]\n\t"
      "v_mov_b32_e32 %[mr], %[lr]\n\t"
      "v_and_b32_e32 %[x4], -16, %[x4]\n\t"
      "v_mov_b32_dpp %[mr], %[lr] row_shr:4 row_mask:0xf bank_mask:0xa\n\t"
      "v_mov_b32_dpp %[lr], %[lr] row_shl:4 row_mask:0xf bank_mask:0x5\n\t"
      "v_and_b32_e32 %[x5], -16, %[x5]\n\t"
      "v_max_f32_dpp v182, %[mr], %[mr] quad_perm:[1,0,3,2] row_mask:0xf bank_mask:0xf\n\t"
      "v_and_b32_e32 %[x6], -16, %[x6]\n\t"
      "v_and_b32_e32 %[x7], -16, %[x7]\n\t"
      "v_max_f32_dpp v182, v182, v182 quad_perm:[2,3,0,1] row_mask:0xf bank_mask:0xf\n\t"
      "s_nop 1\n\t"
      "v_max_f32_dpp v182, v182, v182 row_ror:8 row_mask:0xf bank_mask:0xf\n\t"
      "s_nop 1\n\t"
      "v_max_f32_dpp v182, v182, v182 row_bcast:15 row_mask:0xa bank_mask:0xf\n\t"
      "s_nop 1\n\t"
      "v_max_f32_dpp v182, v182, v182 row_bcast:31 row_mask:0xc bank_mask:0xf\n\t"
      "s_nop 0\n\t"
      "v_readlane_b32 s92, v182, 63\n\t"
      "s_nop 1\n\t"
      "v_subrev_f32_e32 v183, s92, %[mr]\n\t"
      "v_cmp_gt_f32_e32 vcc, s92, %[thr]\n\t"
      "v_exp_f32_e32 v183, v183\n\t"
      "s_cbranch_vccz 3f\n\t"
      "v_mul_f32_e32 v184, v183, %[lr]\n\t"
      "v_mul_f32_dpp v185, v183, %[x0] quad_perm:[0,0,0,0] row_mask:0xf bank_mask:0xf\n\t"
      "v_mul_f32_dpp v186, v183, %[x1] quad_perm:[0,0,0,0] row_mask:0xf bank_mask:0xf\n\t"
      "v_add_f32_dpp v184, v184, v184 quad_perm:[1,0,3,2] row_mask:0xf bank_mask:0xf\n\t"
      "v_fmac_f32_dpp v185, v183, %[x2] quad_perm:[1,1,1,1] row_mask:0xf bank_mask:0xf\n\t"
      "v_fmac_f32_dpp v186, v183, %[x3] quad_perm:[1,1,1,1] row_mask:0xf bank_mask:0xf\n\t"
      "v_add_f32_dpp v184, v184, v184 quad_perm:[2,3,0,1] row_mask:0xf bank_mask:0xf\n\t"
      "v_fmac_f32_dpp v185, v183, %[x4] quad_perm:[2,2,2,2] row_mask:0xf bank_mask:0xf\n\t"
      "v_fmac_f32_dpp v186, v183, %[x5] quad_perm:[2,2,2,2] row_mask:0xf bank_mask:0xf\n\t"
      "v_add_f32_dpp v184, v184, v184 row_ror:8 row_mask:0xf bank_mask:0xf\n\t"
      "v_fmac_f32_dpp v185, v183, %[x6] quad_perm:[3,3,3,3] row_mask:0xf bank_mask:0xf\n\t"
      "v_fmac_f32_dpp v186, v183, %[x7] quad_perm:[3,3,3,3] row_mask:0xf bank_mask:0xf\n\t"
      "v_add_f32_dpp v184, v184, v184 row_bcast:15 row_mask:0xa bank_mask:0xf\n\t"
      "s_nop 0\n\t"
      "v_permlane32_swap_b32_e32 v185, v186\n\t"
      "v_add_f32_dpp v184, v184, v184 row_bcast:31 row_mask:0xc bank_mask:0xf\n\t"
      "v_add_f32_e32 v185, v185, v186\n\t"
      "v_readlane_b32 s93, v184, 63\n\t"
      "s_mov_b32 %[done], 1\n\t"
      "v_add_f32_dpp v185, v185, v185 row_ror:8 row_mask:0xf bank_mask:0xf\n\t"
      "v_rcp_f32_e32 v187, s93\n\t"
      "v_mov_b32_e32 v181, v185\n\t"
      "s_nop 1\n\t"
      "v_permlane16_swap_b32_e32 v185, v181\n\t"
      "v_add_f32_e32 v185, v185, v181\n\t"
      "v_mul_f32_e32 v185, v185, v187\n\t"
      "v_cvt_pk_bf16_f32 v185, v185, v185\n\t"
      "buffer_store_short v185, %[oaddr], %[ors], 0 offen sc1\n\t"
      "3:\n\t"
      "s_nop 1\n\t"
      : [x0] "=&v"(val[0]), [x1] "=&v"(val[1]), [x2] "=&v"(val[2]), [x3] "=&v"(val[3]), [x4] "=&v"(val[4]), [x5] "=&v"(val[5]),
        [x6] "=&v"(val[6]), [x7] "=&v"(val[7]), [mr] "=&v"(Mr), [lr] "=&v"(Lr), [done] "=&s"(done)
      : [base] "v"(base), [oob] "v"(oob), [tag] "v"(tag), [rs] "s"(wrs), [sel1] "v"(sel1), [sel2] "v"(sel2), [thr] "v"(thr),
        [oaddr] "v"(oaddr), [ors] "s"(ors)
      : "memory", "scc", "vcc", "v180", "v181", "v182", "v183", "v184", "v185", "v186", "v187", "v188", "v189", "v190", "v191", "v192", "v193", "v194", "v195", "v196", "v197", "v198", "v199", "v200", "v201", "v202", "v203", "v204", "v205", "v206", "v207", "v208", "v209", "v210", "v211", "v212", "v213", "v214", "v215", "v216", "v217", "v218", "v219", "v220", "v221", "v222", "v223", "v224", "v225", "v226", "v227", "v228", "v229", "v230", "v231", "v232", "v233", "v234", "v235", "v236", "v237", "v238", "v239", "v240", "v241", "v242", "v243", "v244", "v245", "v246", "v247", "v248", "v249", "v250", "v251", "v252", "v253", "v254", "v255", "s64", "s65", "s66", "s67", "s68", "s69", "s70", "s71", "s72", "s73", "s74", "s75", "s76", "s77", "s78", "s79", "s80", "s81", "s82", "s83", "s84", "s85", "s86", "s87", "s88", "s89", "s90", "s91", "s92", "s93");
  STAMP(7);
  if (rank == 0 && lane == 0) __hip_atomic_store((gu32*)(ws + WS_EPOCH + g * 256), tag, __ATOMIC_RELAXED, __HIP_MEMORY_SCOPE_AGENT);
  asm volatile(".Lpf_b:\n\ts_waitcnt vmcnt(0)" ::: "memory");
  if (done) { STAMP_FLUSH(); return; }
  float G = fmaxf(Mr, dpp<0xB1>(Mr));
  G = fmaxf(G, dpp<0x4E>(G));
  G = fmaxf(G, dpp<0x128>(G));
  G = xrow_max(G);
  const float Gs = G;
  const float ws_ = __builtin_amdgcn_exp2f(Mr - Gs);
  float L = ws_ * Lr;
  L += dpp<0xB1>(L); L += dpp<0x4E>(L); L += dpp<0x128>(L); L = xrow_sum(L);
  const float w0 = dpp<0x00>(ws_), w1 = dpp<0x55>(ws_), w2 = dpp<0xAA>(ws_), w3 = dpp<0xFF>(ws_);
#define VAL(i) __uint_as_float(val[i])
  float o0 = fmaf(w1, VAL(2), w0 * VAL(0)) + fmaf(w3, VAL(6), w2 * VAL(4));
  float o1 = fmaf(w1, VAL(3), w0 * VAL(1)) + fmaf(w3, VAL(7), w2 * VAL(5));
  o0 += dpp<0x128>(o0); o1 += dpp<0x128>(o1);
  o0 = xrow_sum(o0); o1 = xrow_sum(o1);
  const int hcol = (g * HG + part) * D + x * 16 + 2 * (lane & 7);
  if (len < SKV) {
    const float Mn = fmaxf(G, NEGBIG2);
    const float a = (L > 0.f) ? __builtin_amdgcn_exp2f(G - Mn) : 0.f;
    const float wm = __builtin_amdgcn_exp2f(NEGBIG2 - Mn);
    float s0 = 0.f, s1 = 0.f;
    for (int j = len; j < SKV; ++j) {
      const unsigned p = (unsigned)bt[j >> 4];
      const unsigned y = *reinterpret_cast<const unsigned*>(v + (size_t)p * (PAGEB / 2) + (size_t)(j & 15) * (TOKB / 2) + hcol);
      s0 += __uint_as_float(y << 16); s1 += __uint_as_float(y & 0xffff0000u);
    }
    L = L * a + wm * (float)(SKV - len); o0 = o0 * a + wm * s0; o1 = o1 * a + wm * s1;
  }
  const float inv = __builtin_amdgcn_rcpf(L);
  if (lane < 8) __hip_atomic_store((gu32*)(out + (hcol >> 1)), pack_bf16(o0 * inv, o1 * inv), __ATOMIC_RELAXED, __HIP_MEMORY_SCOPE_AGENT);
  STAMP_FLUSH();
}

__global__ void __launch_bounds__(256) print_kernel(const unsigned long long* st) {
  unsigned long long tp; asm volatile("s_memrealtime %0\n\ts_waitcnt lgkmcnt(0)" : "=s"(tp) :: "memory");
  __shared__ unsigned long long t0s;
  __shared__ unsigned mn[8], mx[8], sm[8], cn[8];
  if (threadIdx.x == 0) t0s = ~0ull;
  if (threadIdx.x < 8) { mn[threadIdx.x] = ~0u; mx[threadIdx.x] = 0; sm[threadIdx.x] = 0; cn[threadIdx.x] = 0; }
  __syncthreads();
  for (int w = threadIdx.x; w < DIAG_WAVES; w += 256) atomicMin(&t0s, st[(size_t)w * DIAG_N]);
  __syncthreads();
  unsigned long long t0 = t0s;
  for (int w = threadIdx.x; w < DIAG_WAVES; w += 256)
    for (int s = 0; s < 8; ++s) {
      unsigned long long x = st[(size_t)w * DIAG_N + s];
      if (x >= t0 && x - t0 < 1000000) { unsigned d = (unsigned)(x - t0); atomicMin(&mn[s], d); atomicMax(&mx[s], d); atomicAdd(&sm[s], d); atomicAdd(&cn[s], 1u); }
    }
  __syncthreads();
  if (threadIdx.x == 0)
    printf("TL s0[%u %u] s1[%u %u %u] s2[%u %u %u] s3[%u %u %u] s4[%u %u %u] s5[%u %u %u] s6[%u %u %u] s7[%u %u %u] next %u\n",
      sm[0] / cn[0], mx[0], mn[1], sm[1] / cn[1], mx[1], mn[2], sm[2] / cn[2], mx[2], mn[3], sm[3] / cn[3], mx[3], mn[4], sm[4] / cn[4], mx[4],
      mn[5], sm[5] / cn[5], mx[5], mn[6], sm[6] / cn[6], mx[6], mn[7], sm[7] / (cn[7] ? cn[7] : 1), mx[7], (unsigned)(tp - t0));
}

extern "C" void kernel_launch(void* const* d_in, const int* in_sizes, int n_in, void* d_out, int out_size, void* d_ws,
                              size_t ws_size, hipStream_t stream) {
  const unsigned kv_bytes = (unsigned)in_sizes[1] * 2u;
  KArgs a{(const unsigned short*)d_in[0], (const unsigned short*)d_in[1], (const unsigned short*)d_in[2], (const int*)d_in[3],
          (const int*)d_in[4], (unsigned*)d_out, (unsigned char*)d_ws, (unsigned long long)kv_bytes};
  attn_kernel<<<dim3(NWG), dim3(W * 64), 0, stream>>>(a);
  print_kernel<<<1, 256, 0, stream>>>((const unsigned long long*)((char*)d_ws + DIAG_OFF));
}
```

```cpp
#include <hip/hip_runtime.h>
#include <hip/hip_bf16.h>
#include <stdint.h>
#include <float.h>

#define EARLY_WAVES "4"
constexpr unsigned DUE_V1 = 794, DUE_POLL = 1006;

constexpr int H = 32, D = 128, PAGE = 16, NBPS = 256, SKV = NBPS * PAGE;
constexpr int W = 8;
constexpr int NWG = 256;
constexpr int HG = 4;
constexpr int NG = H / HG;
constexpr int RPG = NWG / NG;
constexpr int NU = 16;
constexpr unsigned TOKB = H * D * 2;
constexpr unsigned PAGEB = PAGE * TOKB;
constexpr float C2 = 0.088388347648318440f * 1.4426950408889634f;
constexpr float NEGBIG2 = -1.0e9f * 1.4426950408889634f;

typedef unsigned u32x4 __attribute__((ext_vector_type(4)));
typedef unsigned u32x2 __attribute__((ext_vector_type(2)));
typedef float f32x2 __attribute__((ext_vector_type(2)));
typedef float f32x4 __attribute__((ext_vector_type(4)));
typedef __bf16 bf16x2 __attribute__((ext_vector_type(2)));
#define GAS __attribute__((address_space(1)))
typedef GAS unsigned gu32;

__device__ __forceinline__ float dot2(unsigned a, unsigned b, float c) {
  return __builtin_amdgcn_fdot2_f32_bf16(__builtin_bit_cast(bf16x2, a), __builtin_bit_cast(bf16x2, b), c, false);
}
template <int CTRL> __device__ __forceinline__ float dpp(float x) {
  return __builtin_bit_cast(float, __builtin_amdgcn_mov_dpp(__builtin_bit_cast(int, x), CTRL, 0xf, 0xf, true));
}
__device__ __forceinline__ float rowsum16(float x) {
  x += dpp<0x128>(x); x += dpp<0x124>(x); x += dpp<0x122>(x); x += dpp<0x121>(x); return x;
}
__device__ __forceinline__ float xrow_max(float x) {
  auto s = __builtin_amdgcn_permlane16_swap(__float_as_uint(x), __float_as_uint(x), false, false);
  x = fmaxf(__uint_as_float(s[0]), __uint_as_float(s[1]));
  auto t = __builtin_amdgcn_permlane32_swap(__float_as_uint(x), __float_as_uint(x), false, false);
  return fmaxf(__uint_as_float(t[0]), __uint_as_float(t[1]));
}
__device__ __forceinline__ float xrow_sum(float x) {
  auto s = __builtin_amdgcn_permlane16_swap(__float_as_uint(x), __float_as_uint(x), false, false);
  x = __uint_as_float(s[0]) + __uint_as_float(s[1]);
  auto t = __builtin_amdgcn_permlane32_swap(__float_as_uint(x), __float_as_uint(x), false, false);
  return __uint_as_float(t[0]) + __uint_as_float(t[1]);
}
__device__ __forceinline__ unsigned pack_bf16(float a, float b) {
  bf16x2 r; r[0] = (__bf16)a; r[1] = (__bf16)b; return __builtin_bit_cast(unsigned, r);
}

constexpr unsigned WS_EPOCH = 0, WS_REC = 4096;
constexpr unsigned WS_BYTES = WS_REC + NG * RPG * RPG * 128;
constexpr unsigned SENT = 0xBF800000u;

struct KArgs { const unsigned short* q; const unsigned short* k; const unsigned short* v; const int* bt; const int* lens; unsigned* out; unsigned char* ws; unsigned long long kv_bytes; };
typedef __attribute__((address_space(4))) const KArgs* KArgsP;
typedef __attribute__((address_space(4))) const int* CIntP;
typedef unsigned long long u64;

__global__ void __launch_bounds__(W * 64, W / 4) attn_kernel(KArgs unused_args) {
  __shared__ __attribute__((aligned(16))) float lds_acc[W * HG * D];
  __shared__ __attribute__((aligned(16))) u64 lds_ml[HG * W];
  KArgsP ka = (KArgsP)__builtin_amdgcn_kernarg_segment_ptr();
  asm volatile("" : "+s"(ka));
  const unsigned short* q = ka->q; const unsigned short* k = ka->k; const unsigned short* v = ka->v;
  CIntP bt = (CIntP)ka->bt; CIntP lens = (CIntP)ka->lens; unsigned* out = ka->out; unsigned char* ws = ka->ws;
  unsigned long long kvb = ka->kv_bytes;

  const int x = blockIdx.x & 7, g = (blockIdx.x >> 3) & 7, part = blockIdx.x >> 6;
  const int rank = part * 8 + x;
  const int lane = threadIdx.x & 63, wave = __builtin_amdgcn_readfirstlane(threadIdx.x >> 6);
  const int r = lane >> 4;
  unsigned pfc;
  asm volatile("s_cmp_lg_u32 %2, 0\n\ts_cbranch_scc1 1f\n\ts_getpc_b64 s[98:99]\n\t.Lcode_pc:\n\tv_min_u32_e32 %0, .Lcode_end+128-.Lcode_pc, %1\n\t"
               "global_load_dword %0, %0, s[98:99]\n\t1:" : "=&v"(pfc) : "v"((unsigned)lane * 128u), "s"(wave) : "memory", "scc", "s98", "s99");

  if (threadIdx.x < HG * W) __hip_atomic_store(&lds_ml[threadIdx.x], (u64)SENT << 32, __ATOMIC_RELAXED, __HIP_MEMORY_SCOPE_WORKGROUP);
  unsigned lpb = (unsigned)((wave * 2 + (part >> 1)) * 8 + x) * 4u;
  asm volatile("" : "+s"(lpb));
  asm volatile("s_waitcnt lgkmcnt(0)" : "+s"(bt), "+s"(lens), "+s"(kvb) :: "memory");
  const unsigned kv_bytes = (unsigned)kvb;
  int len = lens[0];
  int pg[2];
#pragma unroll
  for (int i = 0; i < 2; ++i) { pg[i] = *(CIntP)((const __attribute__((address_space(4))) char*)bt + lpb + i * 512); }
  u32x4 pf;
  {
    const unsigned po = WS_REC + (unsigned)(g * RPG + rank) * (RPG * 128) + threadIdx.x * 16u;
    const unsigned oo = (unsigned)((g * HG + part) * D + x * 16) * 2u;
    asm volatile("s_cmp_lt_u32 %5, 4\n\ts_cbranch_scc0 1f\n\tglobal_load_dwordx4 %0, %1, %2" " sc1" "\n\t1:\n\t"
                 "s_cmp_eq_u32 %5, 4\n\ts_cbranch_scc0 2f\n\tglobal_load_dwordx4 %0, %3, %4" " sc1" "\n\t2:"
                 : "=&v"(pf) : "v"(po), "s"(ws), "v"(oo), "s"(out), "s"(wave) : "memory", "scc");
  }
  u32x4 qv;
  unsigned tag;
  asm volatile("global_load_dwordx4 %0, %2, %3\n\tglobal_load_dword %1, %4, %5 sc1"
               : "=&v"(qv), "=&v"(tag) : "v"((unsigned)(g * (HG * D * 2) + lane * 16)), "s"(q), "v"((unsigned)(WS_EPOCH + g * 256)), "s"(ws) : "memory");
  unsigned voff[8];
#pragma unroll
  for (int t = 0; t < 8; ++t) {
    voff[t] = (unsigned)g * (HG * D * 2) + (unsigned)lane * 16 + (unsigned)t * TOKB;
    asm volatile("" : "+v"(voff[t]));
  }
  __builtin_amdgcn_sched_barrier(0);
  __builtin_amdgcn_s_barrier();
  __builtin_amdgcn_sched_barrier(0);
  int nl[2];
  unsigned soff[2], nrec[2];
  auto tile = [&](int i) {
    int R = max(len, 0) - (wave + 8 * i) * 256;
    asm volatile("" : "+s"(R));
    int n = R >= 256 ? 8 : 0;
    unsigned row = (unsigned)(part & 1) * 8u;
    if (R > 0 && R < 256) {
      const int sh = R > 128 ? 3 : R > 64 ? 2 : R > 32 ? 1 : 0;
      const int ts = (((x & 1) << 4) | (part << 2) | (x >> 1)) << sh;
      pg[i] = bt[(wave + 8 * i) * 16 + (ts >> 4)];
      n = min(max(R - ts, 0), 1 << sh);
      row = (unsigned)ts & 15u;
    }
    asm volatile("" : "+s"(n), "+s"(row), "+s"(pg[i]));
    nl[i] = n;
    soff[i] = (unsigned)pg[i] * PAGEB + row * TOKB;
    nrec[i] = min(kv_bytes, soff[i] + (unsigned)n * TOKB);
  };
  asm volatile("" : "+s"(len), "+s"(pg[0]), "+s"(pg[1]));
  tile(0);
  u32x4 K[NU], V[NU];
#define LD8(pre, post, OUTC, X, i, rs) asm volatile(pre \
      "buffer_load_dwordx4 %0, %8, %16, %17 offen nt\n\tbuffer_load_dwordx4 %1, %9, %16, %17 offen nt\n\t" \
      "buffer_load_dwordx4 %2, %10, %16, %17 offen nt\n\tbuffer_load_dwordx4 %3, %11, %16, %17 offen nt\n\t" \
      "buffer_load_dwordx4 %4, %12, %16, %17 offen nt\n\tbuffer_load_dwordx4 %5, %13, %16, %17 offen nt\n\t" \
      "buffer_load_dwordx4 %6, %14, %16, %17 offen nt\n\tbuffer_load_dwordx4 %7, %15, %16, %17 offen nt\n\t" post \
      : OUTC(X[i * 8 + 0]), OUTC(X[i * 8 + 1]), OUTC(X[i * 8 + 2]), OUTC(X[i * 8 + 3]), OUTC(X[i * 8 + 4]), OUTC(X[i * 8 + 5]), OUTC(X[i * 8 + 6]), OUTC(X[i * 8 + 7]) \
      : "v"(voff[0]), "v"(voff[1]), "v"(voff[2]), "v"(voff[3]), "v"(voff[4]), "v"(voff[5]), "v"(voff[6]), "v"(voff[7]), "s"(rs), "s"(soff[i]), "s"(wave) \
      : "memory", "scc")
  const auto krs0 = __builtin_amdgcn_make_buffer_rsrc((void*)k, 0, nrec[0], 0x00020000);
  const auto vrs0 = __builtin_amdgcn_make_buffer_rsrc((void*)v, 0, nrec[0], 0x00020000);
#define PACE(pre, due) asm volatile(pre "7:\n\ts_memrealtime s[98:99]\n\ts_waitcnt lgkmcnt(0)\n\ts_sub_u32 s98, s98, %0\n\t" \
      "s_cmp_ge_u32 s98, %1\n\ts_cbranch_scc1 8f\n\ts_sleep 1\n\ts_branch 7b\n\t8:" :: "s"(t0), "s"(due), "s"(wave) : "memory", "scc", "s98", "s99")
  const unsigned t0 = (unsigned)__builtin_amdgcn_s_memrealtime();
  LD8("", "", "=&v", K, 0, krs0);
  tile(1);
  const auto krs1 = __builtin_amdgcn_make_buffer_rsrc((void*)k, 0, nrec[1], 0x00020000);
  const auto vrs1 = __builtin_amdgcn_make_buffer_rsrc((void*)v, 0, nrec[1], 0x00020000);
  LD8("", "s_barrier", "=&v", K, 1, krs1);
  LD8("s_cmp_lt_u32 %18, " EARLY_WAVES "\n\ts_cbranch_scc0 1f\n\t", "1:", "=&v", V, 0, vrs0);
  len = max(len, 0);
  asm volatile("" : "+s"(len));
  len = min(len, SKV);
  unsigned due2 = ((unsigned)len * DUE_V1) >> 12, due3 = ((unsigned)len * DUE_POLL) >> 12;
  asm volatile("" : "+s"(len), "+s"(due2), "+s"(due3));
  LD8("s_cmp_lt_u32 %18, " EARLY_WAVES "\n\ts_cbranch_scc0 1f\n\t", "1:", "=&v", V, 1, vrs1);
  asm volatile("s_cmp_lt_u32 %18, " EARLY_WAVES "\n\ts_cbranch_scc1 1f\n\ts_waitcnt vmcnt(0)\n\t1:\n\ts_waitcnt vmcnt(16)"
               : "+v"(K[0]), "+v"(K[1]), "+v"(K[2]), "+v"(K[3]), "+v"(K[4]), "+v"(K[5]), "+v"(K[6]), "+v"(K[7]), "+v"(K[8]), "+v"(K[9]),
                 "+v"(K[10]), "+v"(K[11]), "+v"(K[12]), "+v"(K[13]), "+v"(K[14]), "+v"(K[15]), "+v"(qv), "+v"(tag) : "s"(wave) : "scc");
  asm volatile("" :: "v"(pf), "v"(pfc));
  float bias[NU];
#pragma unroll
  for (int u = 0; u < NU; ++u) {
    unsigned bb = ((u & 7) < nl[u >> 3]) ? 0u : 0xff800000u;
    asm volatile("" : "+s"(bb));
    bias[u] = __uint_as_float(bb);
  }
  float sc[NU];
  float m = -FLT_MAX;
#pragma unroll
  for (int u = 0; u < NU; ++u) {
    float d = dot2(K[u][0], qv[0], 0.f);
    d = dot2(K[u][1], qv[1], d); d = dot2(K[u][2], qv[2], d); d = dot2(K[u][3], qv[3], d);
    d = rowsum16(d);
    sc[u] = fmaf(d, C2, bias[u]);
    m = fmaxf(m, sc[u]);
  }
  tag = max(tag + 1u, 1u);
  const float msub = m;
  float l = 0.f;
  f32x2 acc[4];
#pragma unroll
  for (int j = 0; j < 4; ++j) acc[j] = f32x2{0.f, 0.f};
#pragma unroll
  for (int u = 0; u < NU; ++u) { sc[u] = __builtin_amdgcn_exp2f(sc[u] - msub); l += sc[u]; }
  if ((lane & 15) == 0)
    __hip_atomic_store(&lds_ml[r * W + wave], ((u64)__float_as_uint(l) << 32) | __float_as_uint(m), __ATOMIC_RELAXED, __HIP_MEMORY_SCOPE_WORKGROUP);
  const auto wrs = __builtin_amdgcn_make_buffer_rsrc((void*)ws, 0, WS_BYTES, 0x00020000);
  unsigned stoff = WS_REC + (unsigned)(((g * RPG + wave * 4 + r) * RPG + rank) * 128 + (lane & 15) * 8);
  unsigned base = WS_REC + (unsigned)(g * RPG + rank) * (RPG * 128) + (unsigned)lane * 16;
  unsigned recoff = (unsigned)(wave * (HG * D) + lane * 8) * 4u;
  const auto ors = __builtin_amdgcn_make_buffer_rsrc((void*)out, 0, H * D * 2, 0x00020000);
  unsigned oob = 0x80000000u;
  unsigned sel1 = (lane & 1) ? 0x03070105u : 0x06020400u, sel2 = (lane & 2) ? 0x03020706u : 0x05040100u;
  float thr = (len < SKV) ? NEGBIG2 + 256.f : -INFINITY;
  unsigned oaddr = (unsigned)((g * HG + part) * D + x * 16 + 2 * (lane & 7) + (lane >> 5)) * 2u + (unsigned)(lane & 24) * 65536u;
  asm volatile("" : "+v"(stoff), "+v"(base), "+v"(tag), "+v"(recoff), "+v"(oob), "+v"(sel1), "+v"(sel2), "+v"(thr), "+v"(oaddr));
  __builtin_amdgcn_sched_barrier(0);
  LD8("s_cmp_lt_u32 %18, " EARLY_WAVES "\n\ts_cbranch_scc1 1f\n\t", "1:", "+v", V, 0, vrs0);
  PACE("s_cmp_lt_u32 %2, " EARLY_WAVES "\n\ts_cbranch_scc1 8f\n\t", due2);
  LD8("s_cmp_lt_u32 %18, " EARLY_WAVES "\n\ts_cbranch_scc1 1f\n\t", "1:", "+v", V, 1, vrs1);
  __builtin_amdgcn_sched_barrier(0);
  float wt = 0.f;
  unsigned nib = 0;
  auto stats = [&](u64 e) {
    const float mw = __uint_as_float((unsigned)e), lw = __uint_as_float((unsigned)(e >> 32));
    float M = fmaxf(mw, dpp<0xB1>(mw));
    M = fmaxf(M, dpp<0x4E>(M));
    M = fmaxf(M, dpp<0x141>(M));
    const float Ms = M;
    float L = __builtin_amdgcn_exp2f(mw - Ms) * lw;
    L += dpp<0xB1>(L); L += dpp<0x4E>(L); L += dpp<0x141>(L);
    wt = __builtin_amdgcn_exp2f(m - Ms);
    const unsigned Mh = __builtin_amdgcn_readlane(__float_as_uint(M), (wave >> 1) * 16);
    const unsigned Lh = __builtin_amdgcn_readlane(__float_as_uint(L), (wave >> 1) * 16);
    nib = ((lane & 8) ? Lh : Mh) >> ((lane & 7) * 4);
  };
  u64* myml = &lds_ml[r * W + (lane & 7)];
  u64 e0 = __hip_atomic_load(myml, __ATOMIC_RELAXED, __HIP_MEMORY_SCOPE_WORKGROUP);
  const bool early = __all((unsigned)(e0 >> 32) != SENT);
  if (early) stats(e0);
  asm volatile("" : "+v"(wt), "+v"(nib));
  __builtin_amdgcn_sched_barrier(0);
#pragma unroll
  for (int u = 0; u < NU; ++u) {
    asm volatile("s_waitcnt vmcnt(%1)" : "+v"(V[u]) : "n"(NU - 1 - u));
#pragma unroll
    for (int j = 0; j < 4; ++j)
      acc[j] = __builtin_elementwise_fma(f32x2{sc[u], sc[u]}, f32x2{__uint_as_float(V[u][j] << 16), __uint_as_float(V[u][j] & 0xffff0000u)}, acc[j]);
    __builtin_amdgcn_sched_barrier(0);
  }
  asm volatile("" : "+v"(acc[0]), "+v"(acc[1]), "+v"(acc[2]), "+v"(acc[3]));
  if (!early) {
    for (;;) {
      e0 = __hip_atomic_load(myml, __ATOMIC_RELAXED, __HIP_MEMORY_SCOPE_WORKGROUP);
      if (__all((unsigned)(e0 >> 32) != SENT)) break;
      __builtin_amdgcn_s_sleep(2);
    }
    stats(e0);
  }
  {
    float* rec = reinterpret_cast<float*>(reinterpret_cast<char*>(lds_acc) + recoff);
    *reinterpret_cast<f32x4*>(rec) = f32x4{acc[0][0] * wt, acc[0][1] * wt, acc[1][0] * wt, acc[1][1] * wt};
    *reinterpret_cast<f32x4*>(rec + 4) = f32x4{acc[2][0] * wt, acc[2][1] * wt, acc[3][0] * wt, acc[3][1] * wt};
  }
  __syncthreads();
  {
    f32x2 a[W / 2];
#pragma unroll
    for (int w = 0; w < W / 2; ++w) a[w] = f32x2{lds_acc[2 * w * (HG * D) + threadIdx.x], lds_acc[(2 * w + 1) * (HG * D) + threadIdx.x]};
    const f32x2 o2 = (a[0] + a[1]) + (a[2] + a[3]);
    const float o = o2[0] + o2[1];
    __builtin_amdgcn_raw_buffer_store_b64(u32x2{(__float_as_uint(o) & ~15u) | (nib & 15u), tag}, wrs, stoff, 0, 16);
  }
  if (wave != 0) return;
  PACE("", due3);
  unsigned val[8], done;
  float Mr, Lr;
  asm volatile(
      "buffer_load_dwordx4 v[208:211], %[base], %[rs], 0 offen offset:0 sc1\n\t"
      "buffer_load_dwordx4 v[212:215], %[base], %[rs], 0 offen offset:1024 sc1\n\t"
      "buffer_load_dwordx4 v[216:219], %[base], %[rs], 0 offen offset:2048 sc1\n\t"
      "buffer_load_dwordx4 v[220:223], %[base], %[rs], 0 offen offset:3072 sc1\n\t"
      "s_mov_b64 s[64:65], -1\n\t"
      "s_mov_b64 s[66:67], -1\n\t"
      "s_mov_b64 s[68:69], -1\n\t"
      "s_mov_b64 s[70:71], -1\n\t"
      "s_mov_b32 s88, 0x0c0c0400\n\t"
      "s_mov_b32 s89, 0x04000c0c\n\t"
      "s_mov_b32 s90, 0xf0f0f0f0\n\t"
      "s_mov_b32 s91, 0x40000\n\t"
      "s_mov_b32 %[done], 0\n\t"
      "s_sleep 5\n\t"
      "buffer_load_dwordx4 v[224:227], %[base], %[rs], 0 offen offset:0 sc1\n\t"
      "buffer_load_dwordx4 v[228:231], %[base], %[rs], 0 offen offset:1024 sc1\n\t"
      "buffer_load_dwordx4 v[232:235], %[base], %[rs], 0 offen offset:2048 sc1\n\t"
      "buffer_load_dwordx4 v[236:239], %[base], %[rs], 0 offen offset:3072 sc1\n\t"
      "s_sleep 5\n\t"
      "buffer_load_dwordx4 v[240:243], %[base], %[rs], 0 offen offset:0 sc1\n\t"
      "buffer_load_dwordx4 v[244:247], %[base], %[rs], 0 offen offset:1024 sc1\n\t"
      "buffer_load_dwordx4 v[248:251], %[base], %[rs], 0 offen offset:2048 sc1\n\t"
      "buffer_load_dwordx4 v[252:255], %[base], %[rs], 0 offen offset:3072 sc1\n\t"
      "1:\n\t"
      "s_waitcnt vmcnt(8)\n\t"
      "v_cmp_eq_u32_e64 s[72:73], v209, %[tag]\n\t"
      "v_cmp_eq_u32_e64 s[80:81], v211, %[tag]\n\t"
      "v_cmp_eq_u32_e64 s[74:75], v213, %[tag]\n\t"
      "v_cmp_eq_u32_e64 s[82:83], v215, %[tag]\n\t"
      "v_cmp_eq_u32_e64 s[76:77], v217, %[tag]\n\t"
      "v_cmp_eq_u32_e64 s[84:85], v219, %[tag]\n\t"
      "v_cmp_eq_u32_e64 s[78:79], v221, %[tag]\n\t"
      "v_cmp_eq_u32_e64 s[86:87], v223, %[tag]\n\t"
      "s_and_b64 s[72:73], s[72:73], s[80:81]\n\t"
      "s_and_b64 s[74:75], s[74:75], s[82:83]\n\t"
      "s_and_b64 s[76:77], s[76:77], s[84:85]\n\t"
      "s_and_b64 s[78:79], s[78:79], s[86:87]\n\t"
      "v_cndmask_b32_e64 %[x0], %[x0], v208, s[72:73]\n\t"
      "s_andn2_b64 s[64:65], s[64:65], s[72:73]\n\t"
      "v_cndmask_b32_e64 %[x1], %[x1], v210, s[72:73]\n\t"
      "v_cndmask_b32_e64 %[x2], %[x2], v212, s[74:75]\n\t"
      "s_andn2_b64 s[66:67], s[66:67], s[74:75]\n\t"
      "v_cndmask_b32_e64 %[x3], %[x3], v214, s[74:75]\n\t"
      "v_cndmask_b32_e64 %[x4], %[x4], v216, s[76:77]\n\t"
      "s_andn2_b64 s[68:69], s[68:69], s[76:77]\n\t"
      "v_cndmask_b32_e64 %[x5], %[x5], v218, s[76:77]\n\t"
      "v_cndmask_b32_e64 %[x6], %[x6], v220, s[78:79]\n\t"
      "s_andn2_b64 s[70:71], s[70:71], s[78:79]\n\t"
      "v_cndmask_b32_e64 %[x7], %[x7], v222, s[78:79]\n\t"
      "s_or_b64 s[80:81], s[64:65], s[66:67]\n\t"
      "s_or_b64 s[82:83], s[68:69], s[70:71]\n\t"
      "s_or_b64 s[80:81], s[80:81], s[82:83]\n\t"
      "s_cbranch_scc0 2f\n\t"
      "v_cndmask_b32_e64 v204, %[oob], %[base], s[64:65]\n\t"
      "v_cndmask_b32_e64 v205, %[oob], %[base], s[66:67]\n\t"
      "v_cndmask_b32_e64 v206, %[oob], %[base], s[68:69]\n\t"
      "v_cndmask_b32_e64 v207, %[oob], %[base], s[70:71]\n\t"
      "buffer_load_dwordx4 v[208:211], v204, %[rs], 0 offen offset:0 sc1\n\t"
      "buffer_load_dwordx4 v[212:215], v205, %[rs], 0 offen offset:1024 sc1\n\t"
      "buffer_load_dwordx4 v[216:219], v206, %[rs], 0 offen offset:2048 sc1\n\t"
      "buffer_load_dwordx4 v[220:223], v207, %[rs], 0 offen offset:3072 sc1\n\t"
      "s_waitcnt vmcnt(8)\n\t"
      "v_cmp_eq_u32_e64 s[72:73], v225, %[tag]\n\t"
      "v_cmp_eq_u32_e64 s[80:81], v227, %[tag]\n\t"
      "v_cmp_eq_u32_e64 s[74:75], v229, %[tag]\n\t"
      "v_cmp_eq_u32_e64 s[82:83], v231, %[tag]\n\t"
      "v_cmp_eq_u32_e64 s[76:77], v233, %[tag]\n\t"
      "v_cmp_eq_u32_e64 s[84:85], v235, %[tag]\n\t"
      "v_cmp_eq_u32_e64 s[78:79], v237, %[tag]\n\t"
      "v_cmp_eq_u32_e64 s[86:87], v239, %[tag]\n\t"
      "s_and_b64 s[72:73], s[72:73], s[80:81]\n\t"
      "s_and_b64 s[74:75], s[74:75], s[82:83]\n\t"
      "s_and_b64 s[76:77], s[76:77], s[84:85]\n\t"
      "s_and_b64 s[78:79], s[78:79], s[86:87]\n\t"
      "v_cndmask_b32_e64 %[x0], %[x0], v224, s[72:73]\n\t"
      "s_andn2_b64 s[64:65], s[64:65], s[72:73]\n\t"
      "v_cndmask_b32_e64 %[x1], %[x1], v226, s[72:73]\n\t"
      "v_cndmask_b32_e64 %[x2], %[x2], v228, s[74:75]\n\t"
      "s_andn2_b64 s[66:67], s[66:67], s[74:75]\n\t"
      "v_cndmask_b32_e64 %[x3], %[x3], v230, s[74:75]\n\t"
      "v_cndmask_b32_e64 %[x4], %[x4], v232, s[76:77]\n\t"
      "s_andn2_b64 s[68:69], s[68:69], s[76:77]\n\t"
      "v_cndmask_b32_e64 %[x5], %[x5], v234, s[76:77]\n\t"
      "v_cndmask_b32_e64 %[x6], %[x6], v236, s[78:79]\n\t"
      "s_andn2_b64 s[70:71], s[70:71], s[78:79]\n\t"
      "v_cndmask_b32_e64 %[x7], %[x7], v238, s[78:79]\n\t"
      "s_or_b64 s[80:81], s[64:65], s[66:67]\n\t"
      "s_or_b64 s[82:83], s[68:69], s[70:71]\n\t"
      "s_or_b64 s[80:81], s[80:81], s[82:83]\n\t"
      "s_cbranch_scc0 2f\n\t"
      "v_cndmask_b32_e64 v204, %[oob], %[base], s[64:65]\n\t"
      "v_cndmask_b32_e64 v205, %[oob], %[base], s[66:67]\n\t"
      "v_cndmask_b32_e64 v206, %[oob], %[base], s[68:69]\n\t"
      "v_cndmask_b32_e64 v207, %[oob], %[base], s[70:71]\n\t"
      "buffer_load_dwordx4 v[224:227], v204, %[rs], 0 offen offset:0 sc1\n\t"
      "buffer_load_dwordx4 v[228:231], v205, %[rs], 0 offen offset:1024 sc1\n\t"
      "buffer_load_dwordx4 v[232:235], v206, %[rs], 0 offen offset:2048 sc1\n\t"
      "buffer_load_dwordx4 v[236:239], v207, %[rs], 0 offen offset:3072 sc1\n\t"
      "s_waitcnt vmcnt(8)\n\t"
      "v_cmp_eq_u32_e64 s[72:73], v241, %[tag]\n\t"
      "v_cmp_eq_u32_e64 s[80:81], v243, %[tag]\n\t"
      "v_cmp_eq_u32_e64 s[74:75], v245, %[tag]\n\t"
      "v_cmp_eq_u32_e64 s[82:83], v247, %[tag]\n\t"
      "v_cmp_eq_u32_e64 s[76:77], v249, %[tag]\n\t"
      "v_cmp_eq_u32_e64 s[84:85], v251, %[tag]\n\t"
      "v_cmp_eq_u32_e64 s[78:79], v253, %[tag]\n\t"
      "v_cmp_eq_u32_e64 s[86:87], v255, %[tag]\n\t"
      "s_and_b64 s[72:73], s[72:73], s[80:81]\n\t"
      "s_and_b64 s[74:75], s[74:75], s[82:83]\n\t"
      "s_and_b64 s[76:77], s[76:77], s[84:85]\n\t"
      "s_and_b64 s[78:79], s[78:79], s[86:87]\n\t"
      "v_cndmask_b32_e64 %[x0], %[x0], v240, s[72:73]\n\t"
      "s_andn2_b64 s[64:65], s[64:65], s[72:73]\n\t"
      "v_cndmask_b32_e64 %[x1], %[x1], v242, s[72:73]\n\t"
      "v_cndmask_b32_e64 %[x2], %[x2], v244, s[74:75]\n\t"
      "s_andn2_b64 s[66:67], s[66:67], s[74:75]\n\t"
      "v_cndmask_b32_e64 %[x3], %[x3], v246, s[74:75]\n\t"
      "v_cndmask_b32_e64 %[x4], %[x4], v248, s[76:77]\n\t"
      "s_andn2_b64 s[68:69], s[68:69], s[76:77]\n\t"
      "v_cndmask_b32_e64 %[x5], %[x5], v250, s[76:77]\n\t"
      "v_cndmask_b32_e64 %[x6], %[x6], v252, s[78:79]\n\t"
      "s_andn2_b64 s[70:71], s[70:71], s[78:79]\n\t"
      "v_cndmask_b32_e64 %[x7], %[x7], v254, s[78:79]\n\t"
      "s_or_b64 s[80:81], s[64:65], s[66:67]\n\t"
      "s_or_b64 s[82:83], s[68:69], s[70:71]\n\t"
      "s_or_b64 s[80:81], s[80:81], s[82:83]\n\t"
      "s_cbranch_scc0 2f\n\t"
      "v_cndmask_b32_e64 v204, %[oob], %[base], s[64:65]\n\t"
      "v_cndmask_b32_e64 v205, %[oob], %[base], s[66:67]\n\t"
      "v_cndmask_b32_e64 v206, %[oob], %[base], s[68:69]\n\t"
      "v_cndmask_b32_e64 v207, %[oob], %[base], s[70:71]\n\t"
      "buffer_load_dwordx4 v[240:243], v204, %[rs], 0 offen offset:0 sc1\n\t"
      "buffer_load_dwordx4 v[244:247], v205, %[rs], 0 offen offset:1024 sc1\n\t"
      "buffer_load_dwordx4 v[248:251], v206, %[rs], 0 offen offset:2048 sc1\n\t"
      "buffer_load_dwordx4 v[252:255], v207, %[rs], 0 offen offset:3072 sc1\n\t"
      "s_sub_u32 s91, s91, 1\n\t"
      "s_cbranch_scc0 1b\n\t"
      "2:\n\t"
      "v_perm_b32 v196, %[x2], %[x0], s88\n\t"
      "v_perm_b32 v197, %[x6], %[x4], s89\n\t"
      "v_perm_b32 v198, %[x3], %[x1], s88\n\t"
      "v_perm_b32 v199, %[x7], %[x5], s89\n\t"
      "v_or_b32_e32 v196, v196, v197\n\t"
      "v_or_b32_e32 v198, v198, v199\n\t"
      "v_lshlrev_b32_e32 v198, 4, v198\n\t"
      "v_bfi_b32 v196, s90, v198, v196\n\t"
      "v_and_b32_e32 %[x0], -16, %[x0]\n\t"
      "v_and_b32_e32 %[x1], -16, %[x1]\n\t"
      "v_mov_b32_dpp v197, v196 quad_perm:[1,0,3,2] row_mask:0xf bank_mask:0xf\n\t"
      "v_perm_b32 v196, v197, v196, %[sel1]\n\t"
      "v_and_b32_e32 %[x2], -16, %[x2]\n\t"
      "v_and_b32_e32 %[x3], -16, %[x3]\n\t"
      "v_mov_b32_dpp v197, v196 quad_perm:[2,3,0,1] row_mask:0xf bank_mask:0xf\n\t"
      "v_perm_b32 %[lr], v197, v196, %[sel2]\n\t"
      "v_mov_b32_e32 %[mr], %[lr]\n\t"
      "v_and_b32_e32 %[x4], -16, %[x4]\n\t"
      "v_mov_b32_dpp %[mr], %[lr] row_shr:4 row_mask:0xf bank_mask:0xa\n\t"
      "v_mov_b32_dpp %[lr], %[lr] row_shl:4 row_mask:0xf bank_mask:0x5\n\t"
      "v_and_b32_e32 %[x5], -16, %[x5]\n\t"
      "v_max_f32_dpp v198, %[mr], %[mr] quad_perm:[1,0,3,2] row_mask:0xf bank_mask:0xf\n\t"
      "v_and_b32_e32 %[x6], -16, %[x6]\n\t"
      "v_and_b32_e32 %[x7], -16, %[x7]\n\t"
      "v_max_f32_dpp v198, v198, v198 quad_perm:[2,3,0,1] row_mask:0xf bank_mask:0xf\n\t"
      "s_nop 1\n\t"
      "v_max_f32_dpp v198, v198, v198 row_ror:8 row_mask:0xf bank_mask:0xf\n\t"
      "s_nop 1\n\t"
      "v_max_f32_dpp v198, v198, v198 row_bcast:15 row_mask:0xa bank_mask:0xf\n\t"
      "s_nop 1\n\t"
      "v_max_f32_dpp v198, v198, v198 row_bcast:31 row_mask:0xc bank_mask:0xf\n\t"
      "s_nop 0\n\t"
      "v_readlane_b32 s92, v198, 63\n\t"
      "s_nop 1\n\t"
      "v_subrev_f32_e32 v199, s92, %[mr]\n\t"
      "v_cmp_gt_f32_e32 vcc, s92, %[thr]\n\t"
      "v_exp_f32_e32 v199, v199\n\t"
      "s_cbranch_vccz 3f\n\t"
      "v_mul_f32_e32 v200, v199, %[lr]\n\t"
      "v_mul_f32_dpp v201, v199, %[x0] quad_perm:[0,0,0,0] row_mask:0xf bank_mask:0xf\n\t"
      "v_mul_f32_dpp v202, v199, %[x1] quad_perm:[0,0,0,0] row_mask:0xf bank_mask:0xf\n\t"
      "v_add_f32_dpp v200, v200, v200 quad_perm:[1,0,3,2] row_mask:0xf bank_mask:0xf\n\t"
      "v_fmac_f32_dpp v201, v199, %[x2] quad_perm:[1,1,1,1] row_mask:0xf bank_mask:0xf\n\t"
      "v_fmac_f32_dpp v202, v199, %[x3] quad_perm:[1,1,1,1] row_mask:0xf bank_mask:0xf\n\t"
      "v_add_f32_dpp v200, v200, v200 quad_perm:[2,3,0,1] row_mask:0xf bank_mask:0xf\n\t"
      "v_fmac_f32_dpp v201, v199, %[x4] quad_perm:[2,2,2,2] row_mask:0xf bank_mask:0xf\n\t"
      "v_fmac_f32_dpp v202, v199, %[x5] quad_perm:[2,2,2,2] row_mask:0xf bank_mask:0xf\n\t"
      "v_add_f32_dpp v200, v200, v200 row_ror:8 row_mask:0xf bank_mask:0xf\n\t"
      "v_fmac_f32_dpp v201, v199, %[x6] quad_perm:[3,3,3,3] row_mask:0xf bank_mask:0xf\n\t"
      "v_fmac_f32_dpp v202, v199, %[x7] quad_perm:[3,3,3,3] row_mask:0xf bank_mask:0xf\n\t"
      "v_add_f32_dpp v200, v200, v200 row_bcast:15 row_mask:0xa bank_mask:0xf\n\t"
      "s_nop 0\n\t"
      "v_permlane32_swap_b32_e32 v201, v202\n\t"
      "v_add_f32_dpp v200, v200, v200 row_bcast:31 row_mask:0xc bank_mask:0xf\n\t"
      "v_add_f32_e32 v201, v201, v202\n\t"
      "v_readlane_b32 s93, v200, 63\n\t"
      "s_mov_b32 %[done], 1\n\t"
      "v_add_f32_dpp v201, v201, v201 row_ror:8 row_mask:0xf bank_mask:0xf\n\t"
      "v_rcp_f32_e32 v203, s93\n\t"
      "v_mov_b32_e32 v197, v201\n\t"
      "s_nop 1\n\t"
      "v_permlane16_swap_b32_e32 v201, v197\n\t"
      "v_add_f32_e32 v201, v201, v197\n\t"
      "v_mul_f32_e32 v201, v201, v203\n\t"
      "v_cvt_pk_bf16_f32 v201, v201, v201\n\t"
      "buffer_store_short v201, %[oaddr], %[ors], 0 offen sc1\n\t"
      "3:\n\t"
      ".Lcode_end:\n\t"
      "s_nop 1\n\t"
      : [x0] "=&v"(val[0]), [x1] "=&v"(val[1]), [x2] "=&v"(val[2]), [x3] "=&v"(val[3]), [x4] "=&v"(val[4]), [x5] "=&v"(val[5]),
        [x6] "=&v"(val[6]), [x7] "=&v"(val[7]), [mr] "=&v"(Mr), [lr] "=&v"(Lr), [done] "=&s"(done)
      : [base] "v"(base), [oob] "v"(oob), [tag] "v"(tag), [rs] "s"(wrs), [sel1] "v"(sel1), [sel2] "v"(sel2), [thr] "v"(thr),
        [oaddr] "v"(oaddr), [ors] "s"(ors)
      : "memory", "scc", "vcc", "v196", "v197", "v198", "v199", "v200", "v201", "v202", "v203", "v204", "v205", "v206", "v207", "v208", "v209", "v210", "v211", "v212", "v213", "v214", "v215", "v216", "v217", "v218", "v219", "v220", "v221", "v222", "v223", "v224", "v225", "v226", "v227", "v228", "v229", "v230", "v231", "v232", "v233", "v234", "v235", "v236", "v237", "v238", "v239", "v240", "v241", "v242", "v243", "v244", "v245", "v246", "v247", "v248", "v249", "v250", "v251", "v252", "v253", "v254", "v255", "s64", "s65", "s66", "s67", "s68", "s69", "s70", "s71", "s72", "s73", "s74", "s75", "s76", "s77", "s78", "s79", "s80", "s81", "s82", "s83", "s84", "s85", "s86", "s87", "s88", "s89", "s90", "s91", "s92", "s93");
  if (rank == 0 && lane == 0) __hip_atomic_store((gu32*)(ws + WS_EPOCH + g * 256), tag, __ATOMIC_RELAXED, __HIP_MEMORY_SCOPE_AGENT);
  asm volatile("s_waitcnt vmcnt(0)" ::: "memory");
  if (done) return;
  float G = fmaxf(Mr, dpp<0xB1>(Mr));
  G = fmaxf(G, dpp<0x4E>(G));
  G = fmaxf(G, dpp<0x128>(G));
  G = xrow_max(G);
  const float Gs = G;
  const float ws_ = __builtin_amdgcn_exp2f(Mr - Gs);
  float L = ws_ * Lr;
  L += dpp<0xB1>(L); L += dpp<0x4E>(L); L += dpp<0x128>(L); L = xrow_sum(L);
  const float w0 = dpp<0x00>(ws_), w1 = dpp<0x55>(ws_), w2 = dpp<0xAA>(ws_), w3 = dpp<0xFF>(ws_);
#define VAL(i) __uint_as_float(val[i])
  float o0 = fmaf(w1, VAL(2), w0 * VAL(0)) + fmaf(w3, VAL(6), w2 * VAL(4));
  float o1 = fmaf(w1, VAL(3), w0 * VAL(1)) + fmaf(w3, VAL(7), w2 * VAL(5));
  o0 += dpp<0x128>(o0); o1 += dpp<0x128>(o1);
  o0 = xrow_sum(o0); o1 = xrow_sum(o1);
  const int hcol = (g * HG + part) * D + x * 16 + 2 * (lane & 7);
  if (len < SKV) {
    const float Mn = fmaxf(G, NEGBIG2);
    const float a = (L > 0.f) ? __builtin_amdgcn_exp2f(G - Mn) : 0.f;
    const float wm = __builtin_amdgcn_exp2f(NEGBIG2 - Mn);
    float s0 = 0.f, s1 = 0.f;
    for (int j = len; j < SKV; ++j) {
      const unsigned p = (unsigned)bt[j >> 4];
      const unsigned y = *reinterpret_cast<const unsigned*>(v + (size_t)p * (PAGEB / 2) + (size_t)(j & 15) * (TOKB / 2) + hcol);
      s0 += __uint_as_float(y << 16); s1 += __uint_as_float(y & 0xffff0000u);
    }
    L = L * a + wm * (float)(SKV - len); o0 = o0 * a + wm * s0; o1 = o1 * a + wm * s1;
  }
  const float inv = __builtin_amdgcn_rcpf(L);
  if (lane < 8) __hip_atomic_store((gu32*)(out + (hcol >> 1)), pack_bf16(o0 * inv, o1 * inv), __ATOMIC_RELAXED, __HIP_MEMORY_SCOPE_AGENT);
}

extern "C" void kernel_launch(void* const* d_in, const int* in_sizes, int n_in, void* d_out, int out_size, void* d_ws,
                              size_t ws_size, hipStream_t stream) {
  const unsigned kv_bytes = (unsigned)in_sizes[1] * 2u;
  KArgs a{(const unsigned short*)d_in[0], (const unsigned short*)d_in[1], (const unsigned short*)d_in[2], (const int*)d_in[3],
          (const int*)d_in[4], (unsigned*)d_out, (unsigned char*)d_ws, (unsigned long long)kv_bytes};
  attn_kernel<<<dim3(NWG), dim3(W * 64), 0, stream>>>(a);
}
```
